```python
import torch

def _softmax(x, axis=-1):
    return torch.softmax(x, dim=axis)
def _gelu(x):
    return torch.nn.functional.gelu(x, approximate="tanh")
def _silu(x):
    return torch.nn.functional.silu(x)
def _sigmoid(x):
    return torch.sigmoid(x)
def _layernorm(x, axis=-1, eps=1e-5):
    m = x.mean(dim=axis, keepdim=True)
    v = x.var(dim=axis, keepdim=True, unbiased=False)
    return (x - m) / torch.sqrt(v + eps)
def _scatter_add(tgt, idx, src, axis=0):
    return tgt.clone().index_add_(axis, idx, src)
def _segment_sum(src, seg, n):
    out = torch.zeros((n,) + src.shape[1:], dtype=src.dtype)
    return out.index_add_(0, seg, src)
def _pad(x, pw):
    flat = tuple(v for pair in reversed(pw) for v in pair)
    return torch.nn.functional.pad(x, flat)

kernel_name = 'standalone__low_rank_proj_linformer__sampling'

def setup_inputs():

    _w = (lambda a: (lambda t: t.to(torch.bfloat16) if t.is_floating_point() else t)(torch.as_tensor(a)))
    B, H, Sq, Skv, Dh, R = 16, 32, 1, 2048, 128, 64
    return {
        "q": _w((lambda *s: torch.randn(*s, dtype=torch.bfloat16) * 0.8)(B, H, Sq, Dh)),
        "k": _w((lambda *s: torch.randn(*s, dtype=torch.bfloat16) * 0.8)(B, H, Skv, Dh)),
        "v": _w((lambda *s: torch.randn(*s, dtype=torch.bfloat16) * 0.05)(B, H, Skv, Dh)),
        "e_proj": _w((lambda *s: torch.randn(*s, dtype=torch.bfloat16) * 0.8)(R, Skv) * (Skv ** -0.5)),
        "f_proj": _w((lambda *s: torch.randn(*s, dtype=torch.bfloat16) * 0.05)(R, Skv) * (Skv ** -0.5)),
    }

def reference(q, k, v, e_proj, f_proj):
    B, H, Sq, Dh = q.shape
    kp = torch.einsum("rn,bhnd->bhrd", e_proj, k)
    vp = torch.einsum("rn,bhnd->bhrd", f_proj, v)
    scores = torch.einsum("bhqd,bhrd->bhqr", q, kp) * (Dh ** -0.5)
    p = _softmax(scores, axis=-1)
    o = torch.einsum("bhqr,bhrd->bhqd", p, vp)
    return o
```

```cpp
#include <hip/hip_runtime.h>
#include <hip/hip_bf16.h>
#include <stdint.h>

#define LF_NW 4
#define LF_BPW 1
#define LF_ROT 3

namespace lf {
typedef short bf16x8 __attribute__((ext_vector_type(8)));
typedef float f32x4 __attribute__((ext_vector_type(4)));
typedef unsigned u32x4 __attribute__((ext_vector_type(4)));

constexpr int NBH = 512, SKV = 2048, DH = 128, RK = 64;
constexpr unsigned XBYTES = SKV * DH * 2;
constexpr unsigned PBYTES = RK * SKV * 2;

template <int CTRL> __device__ __forceinline__ float dpp(float x) {
  return __builtin_bit_cast(float, __builtin_amdgcn_mov_dpp(__builtin_bit_cast(int, x), CTRL, 0xf, 0xf, true));
}
__device__ __forceinline__ float row16_sum(float x) {
  x += dpp<0x128>(x); x += dpp<0x124>(x); x += dpp<0x122>(x); x += dpp<0x121>(x);
  return x;
}
__device__ __forceinline__ float row16_max(float x) {
  x = fmaxf(x, dpp<0x128>(x)); x = fmaxf(x, dpp<0x124>(x)); x = fmaxf(x, dpp<0x122>(x)); x = fmaxf(x, dpp<0x121>(x));
  return x;
}
__device__ __forceinline__ float xrow_sum(float x) {
  auto s = __builtin_amdgcn_permlane16_swap(__float_as_uint(x), __float_as_uint(x), false, false);
  x = __uint_as_float(s[0]) + __uint_as_float(s[1]);
  auto t = __builtin_amdgcn_permlane32_swap(__float_as_uint(x), __float_as_uint(x), false, false);
  return __uint_as_float(t[0]) + __uint_as_float(t[1]);
}
__device__ __forceinline__ float xrow_max(float x) {
  auto s = __builtin_amdgcn_permlane16_swap(__float_as_uint(x), __float_as_uint(x), false, false);
  x = fmaxf(__uint_as_float(s[0]), __uint_as_float(s[1]));
  auto t = __builtin_amdgcn_permlane32_swap(__float_as_uint(x), __float_as_uint(x), false, false);
  return fmaxf(__uint_as_float(t[0]), __uint_as_float(t[1]));
}
__device__ __forceinline__ float bf_lo(unsigned u) { return __uint_as_float(u << 16); }
__device__ __forceinline__ float bf_hi(unsigned u) { return __uint_as_float(u & 0xffff0000u); }

template <int NW, int BPW, int ROT>
__global__ void __launch_bounds__(NW * 64, 2)
linf_kernel(const unsigned short* __restrict__ q, const unsigned short* __restrict__ k, const unsigned short* __restrict__ v,
            const unsigned short* __restrict__ e, const unsigned short* __restrict__ f, __hip_bfloat16* __restrict__ out
) {
  constexpr int NSTEP = SKV / (32 * NW);
  __shared__ __attribute__((aligned(16))) float lds[BPW * (NW * RK + NW * DH) + NW * RK];
  const int lane = threadIdx.x & 63, wave = __builtin_amdgcn_readfirstlane(threadIdx.x >> 6);
  const int g = lane >> 4, c = lane & 15;
  const int bh0 = blockIdx.x * BPW;
  const __amdgpu_buffer_rsrc_t rk = __builtin_amdgcn_make_buffer_rsrc((void*)(k + (size_t)bh0 * (SKV * DH)), 0, XBYTES * BPW, 0x00020000);
  const __amdgpu_buffer_rsrc_t rv = __builtin_amdgcn_make_buffer_rsrc((void*)(v + (size_t)bh0 * (SKV * DH)), 0, XBYTES * BPW, 0x00020000);
  const __amdgpu_buffer_rsrc_t re = __builtin_amdgcn_make_buffer_rsrc((void*)e, 0, PBYTES, 0x00020000);
  const __amdgpu_buffer_rsrc_t rf = __builtin_amdgcn_make_buffer_rsrc((void*)f, 0, PBYTES, 0x00020000);
  const int xoff = g * 2048 + c * 16;
  const int poff = c * (SKV * 2) + g * 16;
  const int rot = blockIdx.x * ROT;

  u32x4 X[8], P[4];
#define ROW0(s) (((((s) + rot) & (NSTEP - 1)) * NW + wave) * 32)
#define LDX(rs, b, s, i) __builtin_amdgcn_raw_buffer_load_b128(rs, xoff + (i) * 256, (b) * XBYTES + ROW0(s) * 256, 2)
#define LDP(rs, s, t) __builtin_amdgcn_raw_buffer_load_b128(rs, poff, (t) * (16 * SKV * 2) + ROW0(s) * 2, 0)
#pragma unroll
  for (int i = 0; i < 8; ++i) X[i] = LDX(rk, 0, 0, i);
#pragma unroll
  for (int t = 0; t < 4; ++t) P[t] = LDP(re, 0, t);

  f32x4 acc[4][8];
#pragma unroll 1
  for (int ph = 0; ph < 2 * BPW; ++ph) {
    const int b = ph >> 1;
    const bool isv = ph & 1;
#pragma unroll
    for (int t = 0; t < 4; ++t)
#pragma unroll
      for (int j = 0; j < 8; ++j) acc[t][j] = f32x4{0.f, 0.f, 0.f, 0.f};
#pragma unroll 1
    for (int s = 0; s < NSTEP; ++s) {
      const bool last = (s == NSTEP - 1);
      const int ns = last ? 0 : s + 1;
      const bool nv = last ? !isv : isv;
      const int nb = (last && isv) ? b + 1 : b;
      const __amdgpu_buffer_rsrc_t rx = nv ? rv : rk;
      const __amdgpu_buffer_rsrc_t rp = nv ? rf : re;
      u32x4 B[8];
#pragma unroll
      for (int u = 0; u < 4; ++u) {
#pragma unroll
        for (int d = 0; d < 4; ++d) {
          B[2 * d][u]     = __builtin_amdgcn_perm(X[2 * u + 1][d], X[2 * u][d], 0x05040100u);
          B[2 * d + 1][u] = __builtin_amdgcn_perm(X[2 * u + 1][d], X[2 * u][d], 0x07060302u);
        }
        X[2 * u]     = LDX(rx, nb, ns, 2 * u);
        X[2 * u + 1] = LDX(rx, nb, ns, 2 * u + 1);
        __builtin_amdgcn_sched_barrier(0);
      }
#pragma unroll
      for (int t = 0; t < 4; ++t) {
#pragma unroll
        for (int j = 0; j < 8; ++j)
          acc[t][j] = __builtin_amdgcn_mfma_f32_16x16x32_bf16(__builtin_bit_cast(bf16x8, P[t]), __builtin_bit_cast(bf16x8, B[j]), acc[t][j], 0, 0, 0);
        P[t] = LDP(rp, ns, t);
        __builtin_amdgcn_sched_barrier(0);
      }
    }
    float* sc = lds + b * (NW * RK + NW * DH);
    float* op = sc + NW * RK;
    if (!isv) {
      const u32x4 qq = *reinterpret_cast<const u32x4*>(q + (size_t)(bh0 + b) * DH + 8 * c);
      float qf[8];
#pragma unroll
      for (int d = 0; d < 4; ++d) { qf[2 * d] = bf_lo(qq[d]); qf[2 * d + 1] = bf_hi(qq[d]); }
#pragma unroll
      for (int t = 0; t < 4; ++t) {
        f32x4 sv;
#pragma unroll
        for (int r = 0; r < 4; ++r) {
          float x = 0.f;
#pragma unroll
          for (int j = 0; j < 8; ++j) x = fmaf(qf[j], acc[t][j][r], x);
          sv[r] = row16_sum(x);
        }
        if (c == 0) *reinterpret_cast<f32x4*>(sc + wave * RK + 16 * t + 4 * g) = sv;
      }
    } else {
      __syncthreads();
      float s = 0.f;
#pragma unroll
      for (int w = 0; w < NW; ++w) s += sc[w * RK + lane];
      s *= 0.08838834764831845f * 1.4426950408889634f;
      const float m = xrow_max(row16_max(s));
      const float ex = __builtin_amdgcn_exp2f(s - m);
      const float sum = xrow_sum(row16_sum(ex));
      float* pw = lds + BPW * (NW * RK + NW * DH) + wave * RK;
      pw[lane] = ex / sum;
      __builtin_amdgcn_fence(__ATOMIC_RELEASE, "wavefront");
      __builtin_amdgcn_wave_barrier();
      __builtin_amdgcn_fence(__ATOMIC_ACQUIRE, "wavefront");
      float o[8];
#pragma unroll
      for (int j = 0; j < 8; ++j) o[j] = 0.f;
#pragma unroll
      for (int t = 0; t < 4; ++t) {
        const f32x4 pv = *reinterpret_cast<const f32x4*>(pw + 16 * t + 4 * g);
#pragma unroll
        for (int j = 0; j < 8; ++j)
#pragma unroll
          for (int r = 0; r < 4; ++r) o[j] = fmaf(pv[r], acc[t][j][r], o[j]);
      }
#pragma unroll
      for (int j = 0; j < 8; ++j) o[j] = xrow_sum(o[j]);
      if (g == 0) {
        *reinterpret_cast<f32x4*>(op + wave * DH + 8 * c) = f32x4{o[0], o[1], o[2], o[3]};
        *reinterpret_cast<f32x4*>(op + wave * DH + 8 * c + 4) = f32x4{o[4], o[5], o[6], o[7]};
      }
      __syncthreads();
      if (threadIdx.x < DH) {
        float r = 0.f;
#pragma unroll
        for (int w = 0; w < NW; ++w) r += op[w * DH + threadIdx.x];
        out[(size_t)(bh0 + b) * DH + threadIdx.x] = __float2bfloat16(r);
      }
    }
  }
}
}

extern "C" void kernel_launch(void* const* d_in, const int* in_sizes, int n_in, void* d_out, int out_size, void* d_ws, size_t ws_size,
                              hipStream_t stream) {
  (void)in_sizes; (void)n_in; (void)out_size; (void)d_ws; (void)ws_size;
  lf::linf_kernel<LF_NW, LF_BPW, LF_ROT><<<lf::NBH / LF_BPW, LF_NW * 64, 0, stream>>>(
      (const unsigned short*)d_in[0], (const unsigned short*)d_in[1], (const unsigned short*)d_in[2],
      (const unsigned short*)d_in[3], (const unsigned short*)d_in[4], (__hip_bfloat16*)d_out);
}
```

```cpp
#include <hip/hip_runtime.h>
#include <hip/hip_bf16.h>
#include <stdint.h>

#define LF_NW 4
#define LF_BPW 1
#define LF_ROT 3

namespace lf {
typedef short bf16x8 __attribute__((ext_vector_type(8)));
typedef float f32x4 __attribute__((ext_vector_type(4)));
typedef unsigned u32x4 __attribute__((ext_vector_type(4)));

constexpr int NBH = 512, SKV = 2048, DH = 128, RK = 64;
constexpr unsigned XBYTES = SKV * DH * 2;
constexpr unsigned PBYTES = RK * SKV * 2;

template <int CTRL> __device__ __forceinline__ float dpp(float x) {
  return __builtin_bit_cast(float, __builtin_amdgcn_mov_dpp(__builtin_bit_cast(int, x), CTRL, 0xf, 0xf, true));
}
__device__ __forceinline__ float row16_sum(float x) {
  x += dpp<0x128>(x); x += dpp<0x124>(x); x += dpp<0x122>(x); x += dpp<0x121>(x);
  return x;
}
__device__ __forceinline__ float row16_max(float x) {
  x = fmaxf(x, dpp<0x128>(x)); x = fmaxf(x, dpp<0x124>(x)); x = fmaxf(x, dpp<0x122>(x)); x = fmaxf(x, dpp<0x121>(x));
  return x;
}
__device__ __forceinline__ float xrow_sum(float x) {
  auto s = __builtin_amdgcn_permlane16_swap(__float_as_uint(x), __float_as_uint(x), false, false);
  x = __uint_as_float(s[0]) + __uint_as_float(s[1]);
  auto t = __builtin_amdgcn_permlane32_swap(__float_as_uint(x), __float_as_uint(x), false, false);
  return __uint_as_float(t[0]) + __uint_as_float(t[1]);
}
__device__ __forceinline__ float xrow_max(float x) {
  auto s = __builtin_amdgcn_permlane16_swap(__float_as_uint(x), __float_as_uint(x), false, false);
  x = fmaxf(__uint_as_float(s[0]), __uint_as_float(s[1]));
  auto t = __builtin_amdgcn_permlane32_swap(__float_as_uint(x), __float_as_uint(x), false, false);
  return fmaxf(__uint_as_float(t[0]), __uint_as_float(t[1]));
}
__device__ __forceinline__ float bf_lo(unsigned u) { return __uint_as_float(u << 16); }
__device__ __forceinline__ float bf_hi(unsigned u) { return __uint_as_float(u & 0xffff0000u); }

template <int NW, int BPW, int ROT, int XAUX, int PAUX>
__global__ void __launch_bounds__(NW * 64, 2)
linf_kernel(const unsigned short* __restrict__ q, const unsigned short* __restrict__ k, const unsigned short* __restrict__ v,
            const unsigned short* __restrict__ e, const unsigned short* __restrict__ f, __hip_bfloat16* __restrict__ out) {
  constexpr int ND = SKV / (64 * NW);
  constexpr int NS = 2 * ND;
  __shared__ __attribute__((aligned(16))) char smem[NW * 16384 + (BPW * (NW * RK + NW * DH) + NW * RK) * 4];
  float* lds = reinterpret_cast<float*>(smem + NW * 16384);
  const int wave = __builtin_amdgcn_readfirstlane(threadIdx.x >> 6);
  const int bh0 = blockIdx.x * BPW;
  const __amdgpu_buffer_rsrc_t rk = __builtin_amdgcn_make_buffer_rsrc((void*)(k + (size_t)bh0 * (SKV * DH)), 0, XBYTES * BPW, 0x00020000);
  const __amdgpu_buffer_rsrc_t rv = __builtin_amdgcn_make_buffer_rsrc((void*)(v + (size_t)bh0 * (SKV * DH)), 0, XBYTES * BPW, 0x00020000);
  const __amdgpu_buffer_rsrc_t re = __builtin_amdgcn_make_buffer_rsrc((void*)e, 0, PBYTES, 0x00020000);
  const __amdgpu_buffer_rsrc_t rf = __builtin_amdgcn_make_buffer_rsrc((void*)f, 0, PBYTES, 0x00020000);
  const int par = (wave ^ (blockIdx.x >> 3)) & 1;
  int xoff, poff, wr, rd;
  {
    const int lane = threadIdx.x & 63, g = lane >> 4, c = lane & 15;
    xoff = g * 2048 + c * 16;
    poff = (lane >> 3) * 8192 + (lane & 7) * 16;
    wr = wave * 16384 + (lane >> 3) * 256 + (((lane & 7) ^ (lane >> 3)) * 16);
    rd = wave * 16384 + c * 128 + ((g ^ (c >> 1) ^ (par * 4)) * 16);
  }
  const int rot = blockIdx.x * ROT;
  u32x4 X[8], R[4];
#define BLK(S) ((((((S) >> 1) + rot) & (ND - 1)) * NW + wave))
#define XS(S) (((S) / (2 * NS)) * XBYTES + BLK(S) * 16384 + ((((S) ^ par) & 1) * 8192))
#define LDX(rs, so, i) __builtin_amdgcn_raw_buffer_load_b128(rs, xoff + (i) * 256, so, XAUX)
#define PS(T) (((T) & 1) * (32 * 4096) + BLK(T) * 128 + (((T) >= 2 * BPW * NS) ? PBYTES : 0))
#define LDP(rs, so, i) __builtin_amdgcn_raw_buffer_load_b128(rs, poff, (so) + ((i) >> 1) * (16 * 4096) + ((i) & 1) * 4096, PAUX)
#define WRP(T, i, val) *reinterpret_cast<u32x4*>(smem + (wr + ((T) & 3) * 4096) + ((i) >> 1) * 2048 + ((i) & 1) * 128) = (val)
  {
    u32x4 R2[4];
#pragma unroll
    for (int i = 0; i < 8; ++i) X[i] = LDX(rk, XS(0), i);
#pragma unroll
    for (int i = 0; i < 4; ++i) R[i] = LDP(re, PS(0), i);
#pragma unroll
    for (int i = 0; i < 4; ++i) R2[i] = LDP(re, PS(1), i);
#pragma unroll
    for (int i = 0; i < 4; ++i) { WRP(0, i, R[i]); R[i] = LDP(re, PS(2), i); }
#pragma unroll
    for (int i = 0; i < 4; ++i) WRP(1, i, R2[i]);
  }
  f32x4 acc[4][8];
#pragma unroll 1
  for (int ph = 0; ph < 2 * BPW; ++ph) {
    const int b = ph >> 1;
    const bool isv = ph & 1;
#pragma unroll
    for (int t = 0; t < 4; ++t)
#pragma unroll
      for (int j = 0; j < 8; ++j) acc[t][j] = f32x4{0.f, 0.f, 0.f, 0.f};
#pragma unroll 1
    for (int S = ph * NS; S < (ph + 1) * NS; ++S) {
      const __amdgpu_buffer_rsrc_t rx = (((S + 1) / NS) & 1) ? rv : rk;
      const __amdgpu_buffer_rsrc_t rp = (((S + 3) / NS) & 1) ? rf : re;
      const int xs = XS(S + 1), ps = PS(S + 3);
      asm volatile("" : "+v"(xoff));
      u32x4 B[8], P[4];
      const char* rp_lds = smem + (rd ^ (((S & 1) << 6) | ((S & 2) << 12)));
#pragma unroll
      for (int t = 0; t < 4; ++t) P[t] = *reinterpret_cast<const u32x4*>(rp_lds + t * 2048);
      __builtin_amdgcn_sched_barrier(0);
#pragma unroll
      for (int u = 0; u < 4; ++u) {
#pragma unroll
        for (int d = 0; d < 4; ++d) {
          B[2 * d][u]     = __builtin_amdgcn_perm(X[2 * u + 1][d], X[2 * u][d], 0x05040100u);
          B[2 * d + 1][u] = __builtin_amdgcn_perm(X[2 * u + 1][d], X[2 * u][d], 0x07060302u);
        }
        __builtin_amdgcn_sched_barrier(0);
        X[2 * u] = LDX(rx, xs, 2 * u); X[2 * u + 1] = LDX(rx, xs, 2 * u + 1);
        __builtin_amdgcn_sched_barrier(0);
      }
#pragma unroll
      for (int t = 0; t < 4; ++t) {
        WRP(S + 2, t, R[t]);
        R[t] = LDP(rp, ps, t);
#pragma unroll
        for (int j = 0; j < 8; ++j)
          acc[t][j] = __builtin_amdgcn_mfma_f32_16x16x32_bf16(__builtin_bit_cast(bf16x8, P[t]), __builtin_bit_cast(bf16x8, B[j]), acc[t][j], 0, 0, 0);
        __builtin_amdgcn_sched_barrier(0);
      }
    }
    int tid = threadIdx.x;
    asm volatile("" : "+v"(tid));
    const int lane = tid & 63, g = lane >> 4, c = lane & 15;
    float* sc = lds + b * (NW * RK + NW * DH);
    float* op = sc + NW * RK;
    if (!isv) {
      const u32x4 qq = *reinterpret_cast<const u32x4*>(q + (size_t)(bh0 + b) * DH + 8 * c);
      float qf[8];
#pragma unroll
      for (int d = 0; d < 4; ++d) { qf[2 * d] = bf_lo(qq[d]); qf[2 * d + 1] = bf_hi(qq[d]); }
#pragma unroll
      for (int t = 0; t < 4; ++t) {
        f32x4 sv;
#pragma unroll
        for (int r = 0; r < 4; ++r) {
          float x = 0.f;
#pragma unroll
          for (int j = 0; j < 8; ++j) x = fmaf(qf[j], acc[t][j][r], x);
          sv[r] = row16_sum(x);
        }
        if (c == 0) *reinterpret_cast<f32x4*>(sc + wave * RK + 16 * t + 4 * g) = sv;
      }
    } else {
      __syncthreads();
      float s = 0.f;
#pragma unroll
      for (int w = 0; w < NW; ++w) s += sc[w * RK + lane];
      s *= 0.08838834764831845f * 1.4426950408889634f;
      const float m = xrow_max(row16_max(s));
      const float ex = __builtin_amdgcn_exp2f(s - m);
      const float sum = xrow_sum(row16_sum(ex));
      float* pw = lds + BPW * (NW * RK + NW * DH) + wave * RK;
      pw[lane] = ex / sum;
      __builtin_amdgcn_fence(__ATOMIC_RELEASE, "wavefront");
      __builtin_amdgcn_wave_barrier();
      __builtin_amdgcn_fence(__ATOMIC_ACQUIRE, "wavefront");
      float o[8];
#pragma unroll
      for (int j = 0; j < 8; ++j) o[j] = 0.f;
#pragma unroll
      for (int t = 0; t < 4; ++t) {
        const f32x4 pv = *reinterpret_cast<const f32x4*>(pw + 16 * t + 4 * g);
#pragma unroll
        for (int j = 0; j < 8; ++j)
#pragma unroll
          for (int r = 0; r < 4; ++r) o[j] = fmaf(pv[r], acc[t][j][r], o[j]);
      }
#pragma unroll
      for (int j = 0; j < 8; ++j) o[j] = xrow_sum(o[j]);
      if (g == 0) {
        *reinterpret_cast<f32x4*>(op + wave * DH + 8 * c) = f32x4{o[0], o[1], o[2], o[3]};
        *reinterpret_cast<f32x4*>(op + wave * DH + 8 * c + 4) = f32x4{o[4], o[5], o[6], o[7]};
      }
      __syncthreads();
      if (tid < DH) {
        float r = 0.f;
#pragma unroll
        for (int w = 0; w < NW; ++w) r += op[w * DH + tid];
        out[(size_t)(bh0 + b) * DH + tid] = __float2bfloat16(r);
      }
    }
  }
}
}

extern "C" void kernel_launch(void* const* d_in, const int* in_sizes, int n_in, void* d_out, int out_size, void* d_ws, size_t ws_size,
                              hipStream_t stream) {
  (void)in_sizes; (void)n_in; (void)out_size; (void)d_ws; (void)ws_size;
  lf::linf_kernel<LF_NW, LF_BPW, LF_ROT, 2, 0><<<lf::NBH / LF_BPW, LF_NW * 64, 0, stream>>>(
      (const unsigned short*)d_in[0], (const unsigned short*)d_in[1], (const unsigned short*)d_in[2],
      (const unsigned short*)d_in[3], (const unsigned short*)d_in[4], (__hip_bfloat16*)d_out);
}
```

```cpp
#define VLO 0
#define VHI 64
#define KLO 0
#define KHI 0
#include <hip/hip_runtime.h>
#include <hip/hip_bf16.h>
#include <stdint.h>

#define LF_NW 4
#define LF_BPW 1
#define LF_ROT 3

namespace lf {
typedef short bf16x8 __attribute__((ext_vector_type(8)));
typedef float f32x4 __attribute__((ext_vector_type(4)));
typedef unsigned u32x4 __attribute__((ext_vector_type(4)));

constexpr int NBH = 512, SKV = 2048, DH = 128, RK = 64;
constexpr unsigned XBYTES = SKV * DH * 2;
constexpr unsigned PBYTES = RK * SKV * 2;

template <int CTRL> __device__ __forceinline__ float dpp(float x) {
  return __builtin_bit_cast(float, __builtin_amdgcn_mov_dpp(__builtin_bit_cast(int, x), CTRL, 0xf, 0xf, true));
}
__device__ __forceinline__ float row16_sum(float x) {
  x += dpp<0x128>(x); x += dpp<0x124>(x); x += dpp<0x122>(x); x += dpp<0x121>(x);
  return x;
}
__device__ __forceinline__ float row16_max(float x) {
  x = fmaxf(x, dpp<0x128>(x)); x = fmaxf(x, dpp<0x124>(x)); x = fmaxf(x, dpp<0x122>(x)); x = fmaxf(x, dpp<0x121>(x));
  return x;
}
__device__ __forceinline__ float xrow_sum(float x) {
  auto s = __builtin_amdgcn_permlane16_swap(__float_as_uint(x), __float_as_uint(x), false, false);
  x = __uint_as_float(s[0]) + __uint_as_float(s[1]);
  auto t = __builtin_amdgcn_permlane32_swap(__float_as_uint(x), __float_as_uint(x), false, false);
  return __uint_as_float(t[0]) + __uint_as_float(t[1]);
}
__device__ __forceinline__ float xrow_max(float x) {
  auto s = __builtin_amdgcn_permlane16_swap(__float_as_uint(x), __float_as_uint(x), false, false);
  x = fmaxf(__uint_as_float(s[0]), __uint_as_float(s[1]));
  auto t = __builtin_amdgcn_permlane32_swap(__float_as_uint(x), __float_as_uint(x), false, false);
  return fmaxf(__uint_as_float(t[0]), __uint_as_float(t[1]));
}
__device__ __forceinline__ float bf_lo(unsigned u) { return __uint_as_float(u << 16); }
__device__ __forceinline__ float bf_hi(unsigned u) { return __uint_as_float(u & 0xffff0000u); }

template <int NW, int BPW, int ROT, int XAUX, int VAUX>
__device__ __forceinline__ void
linf_body(char* smem, const unsigned short* __restrict__ q, const unsigned short* __restrict__ k, const unsigned short* __restrict__ v,
            const unsigned short* __restrict__ e, const unsigned short* __restrict__ f, __hip_bfloat16* __restrict__ out) {
  constexpr int ND = SKV / (64 * NW);
  constexpr int NS = 2 * ND;
  float* lds = reinterpret_cast<float*>(smem + NW * 16384);
  const int wave = __builtin_amdgcn_readfirstlane(threadIdx.x >> 6);
  const int bh0 = blockIdx.x * BPW;
  const __amdgpu_buffer_rsrc_t rk = __builtin_amdgcn_make_buffer_rsrc((void*)(k + (size_t)bh0 * (SKV * DH)), 0, XBYTES * BPW, 0x00020000);
  const __amdgpu_buffer_rsrc_t rv = __builtin_amdgcn_make_buffer_rsrc((void*)(v + (size_t)bh0 * (SKV * DH)), 0, XBYTES * BPW, 0x00020000);
  const __amdgpu_buffer_rsrc_t re = __builtin_amdgcn_make_buffer_rsrc((void*)e, 0, PBYTES, 0x00020000);
  const __amdgpu_buffer_rsrc_t rf = __builtin_amdgcn_make_buffer_rsrc((void*)f, 0, PBYTES, 0x00020000);
  const int par = (wave ^ (blockIdx.x >> 3)) & 1;
  int xoff, poff, wr, rd;
  {
    const int lane = threadIdx.x & 63, g = lane >> 4, c = lane & 15;
    xoff = g * 2048 + c * 16;
    poff = (lane >> 3) * 8192 + (lane & 7) * 16;
    wr = wave * 16384 + (lane >> 3) * 256 + (((lane & 7) ^ (lane >> 3)) * 16);
    rd = wave * 16384 + c * 128 + ((g ^ (c >> 1) ^ (par * 4)) * 16);
  }
  const int rot = blockIdx.x * ROT;
  u32x4 X[8], R[4];
#define BLK(S) ((((((S) >> 1) + rot) & (ND - 1)) * NW + wave))
#define XS(S) (((S) / (2 * NS)) * XBYTES + BLK(S) * 16384 + ((((S) ^ par) & 1) * 8192))
#define LDX(rs, so, i) __builtin_amdgcn_raw_buffer_load_b128(rs, xoff + (i) * 256, so, XAUX)
#define LDV(rs, so, i) __builtin_amdgcn_raw_buffer_load_b128(rs, xoff + (i) * 256, so, VAUX)
#define PAUX 0
#define PS(T) (((T) & 1) * (32 * 4096) + BLK(T) * 128 + (((T) >= 2 * BPW * NS) ? PBYTES : 0))
#define LDP(rs, so, i) __builtin_amdgcn_raw_buffer_load_b128(rs, poff, (so) + ((i) >> 1) * (16 * 4096) + ((i) & 1) * 4096, PAUX)
#define WRP(T, i, val) *reinterpret_cast<u32x4*>(smem + (wr + ((T) & 3) * 4096) + ((i) >> 1) * 2048 + ((i) & 1) * 128) = (val)
  {
    u32x4 R2[4];
#pragma unroll
    for (int i = 0; i < 8; ++i) X[i] = LDX(rk, XS(0), i);
#pragma unroll
    for (int i = 0; i < 4; ++i) R[i] = LDP(re, PS(0), i);
#pragma unroll
    for (int i = 0; i < 4; ++i) R2[i] = LDP(re, PS(1), i);
#pragma unroll
    for (int i = 0; i < 4; ++i) { WRP(0, i, R[i]); R[i] = LDP(re, PS(2), i); }
#pragma unroll
    for (int i = 0; i < 4; ++i) WRP(1, i, R2[i]);
  }
  f32x4 acc[4][8];
#pragma unroll
  for (int ph = 0; ph < 2 * BPW; ++ph) {
    const int b = ph >> 1;
    const bool isv = ph & 1;
#pragma unroll
    for (int t = 0; t < 4; ++t)
#pragma unroll
      for (int j = 0; j < 8; ++j) acc[t][j] = f32x4{0.f, 0.f, 0.f, 0.f};
#pragma unroll 1
    for (int S = ph * NS; S < (ph + 1) * NS; ++S) {
      const __amdgpu_buffer_rsrc_t rx = (((S + 1) / NS) & 1) ? rv : rk;
      const __amdgpu_buffer_rsrc_t rp = (((S + 3) / NS) & 1) ? rf : re;
      const int xs = XS(S + 1), ps = PS(S + 3);
      asm volatile("" : "+v"(xoff));
      u32x4 B[8], P[4];
      const char* rp_lds = smem + (rd ^ (((S & 1) << 6) | ((S & 2) << 12)));
#pragma unroll
      for (int t = 0; t < 4; ++t) P[t] = *reinterpret_cast<const u32x4*>(rp_lds + t * 2048);
      __builtin_amdgcn_sched_barrier(0);
#pragma unroll
      for (int u = 0; u < 4; ++u) {
#pragma unroll
        for (int d = 0; d < 4; ++d) {
          B[2 * d][u]     = __builtin_amdgcn_perm(X[2 * u + 1][d], X[2 * u][d], 0x05040100u);
          B[2 * d + 1][u] = __builtin_amdgcn_perm(X[2 * u + 1][d], X[2 * u][d], 0x07060302u);
        }
        __builtin_amdgcn_sched_barrier(0);
        if (isv) { X[2 * u] = LDV(rx, xs, 2 * u); X[2 * u + 1] = LDV(rx, xs, 2 * u + 1); } else { X[2 * u] = LDX(rx, xs, 2 * u); X[2 * u + 1] = LDX(rx, xs, 2 * u + 1); }
        __builtin_amdgcn_sched_barrier(0);
      }
#pragma unroll
      for (int t = 0; t < 4; ++t) {
        WRP(S + 2, t, R[t]);
        R[t] = LDP(rp, ps, t);
#pragma unroll
        for (int j = 0; j < 8; ++j)
          acc[t][j] = __builtin_amdgcn_mfma_f32_16x16x32_bf16(__builtin_bit_cast(bf16x8, P[t]), __builtin_bit_cast(bf16x8, B[j]), acc[t][j], 0, 0, 0);
        __builtin_amdgcn_sched_barrier(0);
      }
    }
    int tid = threadIdx.x;
    asm volatile("" : "+v"(tid));
    const int lane = tid & 63, g = lane >> 4, c = lane & 15;
    float* sc = lds + b * (NW * RK + NW * DH);
    float* op = sc + NW * RK;
    if (!isv) {
      const u32x4 qq = *reinterpret_cast<const u32x4*>(q + (size_t)(bh0 + b) * DH + 8 * c);
      float qf[8];
#pragma unroll
      for (int d = 0; d < 4; ++d) { qf[2 * d] = bf_lo(qq[d]); qf[2 * d + 1] = bf_hi(qq[d]); }
#pragma unroll
      for (int t = 0; t < 4; ++t) {
        f32x4 sv;
#pragma unroll
        for (int r = 0; r < 4; ++r) {
          float x = 0.f;
#pragma unroll
          for (int j = 0; j < 8; ++j) x = fmaf(qf[j], acc[t][j][r], x);
          sv[r] = row16_sum(x);
        }
        if (c == 0) *reinterpret_cast<f32x4*>(sc + wave * RK + 16 * t + 4 * g) = sv;
      }
    } else {
      __syncthreads();
      float s = 0.f;
#pragma unroll
      for (int w = 0; w < NW; ++w) s += sc[w * RK + lane];
      s *= 0.08838834764831845f * 1.4426950408889634f;
      const float m = xrow_max(row16_max(s));
      const float ex = __builtin_amdgcn_exp2f(s - m);
      const float sum = xrow_sum(row16_sum(ex));
      float* pw = lds + BPW * (NW * RK + NW * DH) + wave * RK;
      pw[lane] = ex / sum;
      __builtin_amdgcn_fence(__ATOMIC_RELEASE, "wavefront");
      __builtin_amdgcn_wave_barrier();
      __builtin_amdgcn_fence(__ATOMIC_ACQUIRE, "wavefront");
      float o[8];
#pragma unroll
      for (int j = 0; j < 8; ++j) o[j] = 0.f;
#pragma unroll
      for (int t = 0; t < 4; ++t) {
        const f32x4 pv = *reinterpret_cast<const f32x4*>(pw + 16 * t + 4 * g);
#pragma unroll
        for (int j = 0; j < 8; ++j)
#pragma unroll
          for (int r = 0; r < 4; ++r) o[j] = fmaf(pv[r], acc[t][j][r], o[j]);
      }
#pragma unroll
      for (int j = 0; j < 8; ++j) o[j] = xrow_sum(o[j]);
      if (g == 0) {
        *reinterpret_cast<f32x4*>(op + wave * DH + 8 * c) = f32x4{o[0], o[1], o[2], o[3]};
        *reinterpret_cast<f32x4*>(op + wave * DH + 8 * c + 4) = f32x4{o[4], o[5], o[6], o[7]};
      }
      __syncthreads();
      if (tid < DH) {
        float r = 0.f;
#pragma unroll
        for (int w = 0; w < NW; ++w) r += op[w * DH + tid];
        out[(size_t)(bh0 + b) * DH + tid] = __float2bfloat16(r);
      }
    }
  }
}

__global__ void __launch_bounds__(256, 2)
linf_kernel(const unsigned short* __restrict__ q, const unsigned short* __restrict__ k, const unsigned short* __restrict__ v,
            const unsigned short* __restrict__ e, const unsigned short* __restrict__ f, __hip_bfloat16* __restrict__ out) {
  constexpr int vlo = VLO, vhi = VHI, klo = KLO, khi = KHI;
  __shared__ __attribute__((aligned(16))) char smem[4 * 16384 + ((4 * RK + 4 * DH) + 4 * RK) * 4];
  const bool vdef = (int)blockIdx.x >= vlo && (int)blockIdx.x < vhi, kdef = (int)blockIdx.x >= klo && (int)blockIdx.x < khi;
  if (kdef && vdef) linf_body<4, 1, 3, 0, 0>(smem, q, k, v, e, f, out);
  else if (kdef) linf_body<4, 1, 3, 0, 2>(smem, q, k, v, e, f, out);
  else if (vdef) linf_body<4, 1, 3, 2, 0>(smem, q, k, v, e, f, out);
  else linf_body<4, 1, 3, 2, 2>(smem, q, k, v, e, f, out);
}
}

extern "C" void kernel_launch(void* const* d_in, const int* in_sizes, int n_in, void* d_out, int out_size, void* d_ws, size_t ws_size,
                              hipStream_t stream) {
  (void)in_sizes; (void)n_in; (void)out_size; (void)d_ws; (void)ws_size;
  lf::linf_kernel<<<lf::NBH, 256, 0, stream>>>(
      (const unsigned short*)d_in[0], (const unsigned short*)d_in[1], (const unsigned short*)d_in[2],
      (const unsigned short*)d_in[3], (const unsigned short*)d_in[4], (__hip_bfloat16*)d_out);
}
```

```cpp
#include <hip/hip_runtime.h>
#include <hip/hip_bf16.h>
#include <stdint.h>

#define LF_NW 4
#define LF_BPW 1
#define LF_ROT 3

namespace lf {
typedef short bf16x8 __attribute__((ext_vector_type(8)));
typedef float f32x4 __attribute__((ext_vector_type(4)));
typedef unsigned u32x4 __attribute__((ext_vector_type(4)));

constexpr int NBH = 512, SKV = 2048, DH = 128, RK = 64;
constexpr unsigned XBYTES = SKV * DH * 2;
constexpr unsigned PBYTES = RK * SKV * 2;

template <int CTRL> __device__ __forceinline__ float dpp(float x) {
  return __builtin_bit_cast(float, __builtin_amdgcn_mov_dpp(__builtin_bit_cast(int, x), CTRL, 0xf, 0xf, true));
}
__device__ __forceinline__ float row16_sum(float x) {
  x += dpp<0x128>(x); x += dpp<0x124>(x); x += dpp<0x122>(x); x += dpp<0x121>(x);
  return x;
}
__device__ __forceinline__ float row16_max(float x) {
  x = fmaxf(x, dpp<0x128>(x)); x = fmaxf(x, dpp<0x124>(x)); x = fmaxf(x, dpp<0x122>(x)); x = fmaxf(x, dpp<0x121>(x));
  return x;
}
__device__ __forceinline__ float xrow_sum(float x) {
  auto s = __builtin_amdgcn_permlane16_swap(__float_as_uint(x), __float_as_uint(x), false, false);
  x = __uint_as_float(s[0]) + __uint_as_float(s[1]);
  auto t = __builtin_amdgcn_permlane32_swap(__float_as_uint(x), __float_as_uint(x), false, false);
  return __uint_as_float(t[0]) + __uint_as_float(t[1]);
}
__device__ __forceinline__ float xrow_max(float x) {
  auto s = __builtin_amdgcn_permlane16_swap(__float_as_uint(x), __float_as_uint(x), false, false);
  x = fmaxf(__uint_as_float(s[0]), __uint_as_float(s[1]));
  auto t = __builtin_amdgcn_permlane32_swap(__float_as_uint(x), __float_as_uint(x), false, false);
  return fmaxf(__uint_as_float(t[0]), __uint_as_float(t[1]));
}
__device__ __forceinline__ float bf_lo(unsigned u) { return __uint_as_float(u << 16); }
__device__ __forceinline__ float bf_hi(unsigned u) { return __uint_as_float(u & 0xffff0000u); }

template <int NW, int BPW, int ROT, int XAUX, int PAUX>
__global__ void __launch_bounds__(NW * 64, 2)
linf_kernel(const unsigned short* __restrict__ q, const unsigned short* __restrict__ k, const unsigned short* __restrict__ v,
            const unsigned short* __restrict__ e, const unsigned short* __restrict__ f, __hip_bfloat16* __restrict__ out) {
  constexpr int ND = SKV / (64 * NW);
  constexpr int NS = 2 * ND;
  __shared__ __attribute__((aligned(16))) char smem[NW * 16384 + (BPW * (NW * RK + NW * DH) + NW * RK) * 4];
  float* lds = reinterpret_cast<float*>(smem + NW * 16384);
  const unsigned long long ta = __builtin_amdgcn_s_memrealtime();
  __builtin_amdgcn_sched_barrier(0);
  asm volatile("s_waitcnt lgkmcnt(0)" :: "s"(k), "s"(ta) : "memory");
  __builtin_amdgcn_sched_barrier(0);
  const unsigned long long tb = __builtin_amdgcn_s_memrealtime();
  __builtin_amdgcn_sched_barrier(0);
  const int wave = __builtin_amdgcn_readfirstlane(threadIdx.x >> 6);
  const int bh0 = blockIdx.x * BPW;
  const __amdgpu_buffer_rsrc_t rk = __builtin_amdgcn_make_buffer_rsrc((void*)(k + (size_t)bh0 * (SKV * DH)), 0, XBYTES * BPW, 0x00020000);
  const __amdgpu_buffer_rsrc_t rv = __builtin_amdgcn_make_buffer_rsrc((void*)(v + (size_t)bh0 * (SKV * DH)), 0, XBYTES * BPW, 0x00020000);
  const __amdgpu_buffer_rsrc_t re = __builtin_amdgcn_make_buffer_rsrc((void*)e, 0, PBYTES, 0x00020000);
  const __amdgpu_buffer_rsrc_t rf = __builtin_amdgcn_make_buffer_rsrc((void*)f, 0, PBYTES, 0x00020000);
  const int par = (wave ^ (blockIdx.x >> 3)) & 1;
  int xoff, poff, wr, rd;
  {
    const int lane = threadIdx.x & 63, g = lane >> 4, c = lane & 15;
    xoff = g * 2048 + c * 16;
    poff = (lane >> 3) * 8192 + (lane & 7) * 16;
    wr = wave * 16384 + (lane >> 3) * 256 + (((lane & 7) ^ (lane >> 3)) * 16);
    rd = wave * 16384 + c * 128 + ((g ^ (c >> 1) ^ (par * 4)) * 16);
  }
  const int rot = blockIdx.x * ROT;
  u32x4 X[8], R[4];
#define BLK(S) ((((((S) >> 1) + rot) & (ND - 1)) * NW + wave))
#define XS(S) (((S) / (2 * NS)) * XBYTES + BLK(S) * 16384 + ((((S) ^ par) & 1) * 8192))
#define LDX(rs, so, i) __builtin_amdgcn_raw_buffer_load_b128(rs, xoff + (i) * 256, so, XAUX)
#define PS(T) (((T) & 1) * (32 * 4096) + BLK(T) * 128 + (((T) >= 2 * BPW * NS) ? PBYTES : 0))
#define LDP(rs, so, i) __builtin_amdgcn_raw_buffer_load_b128(rs, poff, (so) + ((i) >> 1) * (16 * 4096) + ((i) & 1) * 4096, PAUX)
#define WRP(T, i, val) *reinterpret_cast<u32x4*>(smem + (wr + ((T) & 3) * 4096) + ((i) >> 1) * 2048 + ((i) & 1) * 128) = (val)
  {
    u32x4 R2[4];
#pragma unroll
    for (int i = 0; i < 8; ++i) X[i] = LDX(rk, XS(0), i);
#pragma unroll
    for (int i = 0; i < 4; ++i) R[i] = LDP(re, PS(0), i);
#pragma unroll
    for (int i = 0; i < 4; ++i) R2[i] = LDP(re, PS(1), i);
#pragma unroll
    for (int i = 0; i < 4; ++i) { WRP(0, i, R[i]); R[i] = LDP(re, PS(2), i); }
#pragma unroll
    for (int i = 0; i < 4; ++i) WRP(1, i, R2[i]);
  }
  f32x4 acc[4][8];
#pragma unroll 1
  for (int ph = 0; ph < 2 * BPW; ++ph) {
    const int b = ph >> 1;
    const bool isv = ph & 1;
#pragma unroll
    for (int t = 0; t < 4; ++t)
#pragma unroll
      for (int j = 0; j < 8; ++j) acc[t][j] = f32x4{0.f, 0.f, 0.f, 0.f};
#pragma unroll 1
    for (int S = ph * NS; S < (ph + 1) * NS; ++S) {
      const __amdgpu_buffer_rsrc_t rx = (((S + 1) / NS) & 1) ? rv : rk;
      const __amdgpu_buffer_rsrc_t rp = (((S + 3) / NS) & 1) ? rf : re;
      const int xs = XS(S + 1), ps = PS(S + 3);
      asm volatile("" : "+v"(xoff));
      u32x4 B[8], P[4];
      const char* rp_lds = smem + (rd ^ (((S & 1) << 6) | ((S & 2) << 12)));
#pragma unroll
      for (int t = 0; t < 4; ++t) P[t] = *reinterpret_cast<const u32x4*>(rp_lds + t * 2048);
      __builtin_amdgcn_sched_barrier(0);
#pragma unroll
      for (int u = 0; u < 4; ++u) {
#pragma unroll
        for (int d = 0; d < 4; ++d) {
          B[2 * d][u]     = __builtin_amdgcn_perm(X[2 * u + 1][d], X[2 * u][d], 0x05040100u);
          B[2 * d + 1][u] = __builtin_amdgcn_perm(X[2 * u + 1][d], X[2 * u][d], 0x07060302u);
        }
        __builtin_amdgcn_sched_barrier(0);
        X[2 * u] = LDX(rx, xs, 2 * u); X[2 * u + 1] = LDX(rx, xs, 2 * u + 1);
        __builtin_amdgcn_sched_barrier(0);
      }
#pragma unroll
      for (int t = 0; t < 4; ++t) {
        WRP(S + 2, t, R[t]);
        R[t] = LDP(rp, ps, t);
#pragma unroll
        for (int j = 0; j < 8; ++j)
          acc[t][j] = __builtin_amdgcn_mfma_f32_16x16x32_bf16(__builtin_bit_cast(bf16x8, P[t]), __builtin_bit_cast(bf16x8, B[j]), acc[t][j], 0, 0, 0);
        __builtin_amdgcn_sched_barrier(0);
      }
    }
    int tid = threadIdx.x;
    asm volatile("" : "+v"(tid));
    const int lane = tid & 63, g = lane >> 4, c = lane & 15;
    float* sc = lds + b * (NW * RK + NW * DH);
    float* op = sc + NW * RK;
    if (!isv) {
      const u32x4 qq = *reinterpret_cast<const u32x4*>(q + (size_t)(bh0 + b) * DH + 8 * c);
      float qf[8];
#pragma unroll
      for (int d = 0; d < 4; ++d) { qf[2 * d] = bf_lo(qq[d]); qf[2 * d + 1] = bf_hi(qq[d]); }
#pragma unroll
      for (int t = 0; t < 4; ++t) {
        f32x4 sv;
#pragma unroll
        for (int r = 0; r < 4; ++r) {
          float x = 0.f;
#pragma unroll
          for (int j = 0; j < 8; ++j) x = fmaf(qf[j], acc[t][j][r], x);
          sv[r] = row16_sum(x);
        }
        if (c == 0) *reinterpret_cast<f32x4*>(sc + wave * RK + 16 * t + 4 * g) = sv;
      }
    } else {
      __syncthreads();
      float s = 0.f;
#pragma unroll
      for (int w = 0; w < NW; ++w) s += sc[w * RK + lane];
      s *= 0.08838834764831845f * 1.4426950408889634f;
      const float m = xrow_max(row16_max(s));
      const float ex = __builtin_amdgcn_exp2f(s - m);
      const float sum = xrow_sum(row16_sum(ex));
      float* pw = lds + BPW * (NW * RK + NW * DH) + wave * RK;
      pw[lane] = ex / sum;
      __builtin_amdgcn_fence(__ATOMIC_RELEASE, "wavefront");
      __builtin_amdgcn_wave_barrier();
      __builtin_amdgcn_fence(__ATOMIC_ACQUIRE, "wavefront");
      float o[8];
#pragma unroll
      for (int j = 0; j < 8; ++j) o[j] = 0.f;
#pragma unroll
      for (int t = 0; t < 4; ++t) {
        const f32x4 pv = *reinterpret_cast<const f32x4*>(pw + 16 * t + 4 * g);
#pragma unroll
        for (int j = 0; j < 8; ++j)
#pragma unroll
          for (int r = 0; r < 4; ++r) o[j] = fmaf(pv[r], acc[t][j][r], o[j]);
      }
#pragma unroll
      for (int j = 0; j < 8; ++j) o[j] = xrow_sum(o[j]);
      if (g == 0) {
        *reinterpret_cast<f32x4*>(op + wave * DH + 8 * c) = f32x4{o[0], o[1], o[2], o[3]};
        *reinterpret_cast<f32x4*>(op + wave * DH + 8 * c + 4) = f32x4{o[4], o[5], o[6], o[7]};
      }
      __syncthreads();
      if (tid < DH) {
        float r = 0.f;
#pragma unroll
        for (int w = 0; w < NW; ++w) r += op[w * DH + tid];
        out[(size_t)(bh0 + b) * DH + tid] = __float2bfloat16(r);
      }
    }
  }
  const unsigned long long tt = ta + 12000;
  if (blockIdx.x == 0) while (__builtin_amdgcn_s_memrealtime() < tt) __builtin_amdgcn_s_sleep(4);
  (void)tb;
}
}

extern "C" void kernel_launch(void* const* d_in, const int* in_sizes, int n_in, void* d_out, int out_size, void* d_ws, size_t ws_size,
                              hipStream_t stream) {
  (void)in_sizes; (void)n_in; (void)out_size; (void)d_ws; (void)ws_size;
  lf::linf_kernel<LF_NW, LF_BPW, LF_ROT, 2, 0><<<lf::NBH / LF_BPW, LF_NW * 64, 0, stream>>>(
      (const unsigned short*)d_in[0], (const unsigned short*)d_in[1], (const unsigned short*)d_in[2],
      (const unsigned short*)d_in[3], (const unsigned short*)d_in[4], (__hip_bfloat16*)d_out);
}
```

```cpp
#define PRIO 1
#define SWZ 1
#define PREF 0
#define PREFS 0
#define XF 1
#include <hip/hip_runtime.h>
#include <hip/hip_bf16.h>
#include <stdint.h>

#define LF_NW 4
#define LF_BPW 1
#define LF_ROT 3

namespace lf {
typedef short bf16x8 __attribute__((ext_vector_type(8)));
typedef float f32x4 __attribute__((ext_vector_type(4)));
typedef unsigned u32x4 __attribute__((ext_vector_type(4)));

constexpr int NBH = 512, SKV = 2048, DH = 128, RK = 64;
constexpr unsigned XBYTES = SKV * DH * 2;
constexpr unsigned PBYTES = RK * SKV * 2;

template <int CTRL> __device__ __forceinline__ float dpp(float x) {
  return __builtin_bit_cast(float, __builtin_amdgcn_mov_dpp(__builtin_bit_cast(int, x), CTRL, 0xf, 0xf, true));
}
__device__ __forceinline__ float row16_sum(float x) {
  x += dpp<0x128>(x); x += dpp<0x124>(x); x += dpp<0x122>(x); x += dpp<0x121>(x);
  return x;
}
__device__ __forceinline__ float row16_max(float x) {
  x = fmaxf(x, dpp<0x128>(x)); x = fmaxf(x, dpp<0x124>(x)); x = fmaxf(x, dpp<0x122>(x)); x = fmaxf(x, dpp<0x121>(x));
  return x;
}
__device__ __forceinline__ float xrow_sum(float x) {
  auto s = __builtin_amdgcn_permlane16_swap(__float_as_uint(x), __float_as_uint(x), false, false);
  x = __uint_as_float(s[0]) + __uint_as_float(s[1]);
  auto t = __builtin_amdgcn_permlane32_swap(__float_as_uint(x), __float_as_uint(x), false, false);
  return __uint_as_float(t[0]) + __uint_as_float(t[1]);
}
__device__ __forceinline__ float xrow_max(float x) {
  auto s = __builtin_amdgcn_permlane16_swap(__float_as_uint(x), __float_as_uint(x), false, false);
  x = fmaxf(__uint_as_float(s[0]), __uint_as_float(s[1]));
  auto t = __builtin_amdgcn_permlane32_swap(__float_as_uint(x), __float_as_uint(x), false, false);
  return fmaxf(__uint_as_float(t[0]), __uint_as_float(t[1]));
}
__device__ __forceinline__ float bf_lo(unsigned u) { return __uint_as_float(u << 16); }
__device__ __forceinline__ float bf_hi(unsigned u) { return __uint_as_float(u & 0xffff0000u); }

template <int NW, int BPW, int ROT, int XAUX, int PAUX>
__global__ void __launch_bounds__(NW * 64, 2)
linf_kernel(const unsigned short* __restrict__ q, const unsigned short* __restrict__ k, const unsigned short* __restrict__ v,
            const unsigned short* __restrict__ e, const unsigned short* __restrict__ f, __hip_bfloat16* __restrict__ out) {
  constexpr int ND = SKV / (64 * NW);
  constexpr int NS = 2 * ND;
  __shared__ __attribute__((aligned(16))) char smem[NW * 16384 + (BPW * (NW * RK + NW * DH) + NW * RK) * 4];
  float* lds = reinterpret_cast<float*>(smem + NW * 16384);
  const int wave = __builtin_amdgcn_readfirstlane(threadIdx.x >> 6);
  const int bh0 = (SWZ == 1 ? (blockIdx.x & 7) * 64 + (blockIdx.x >> 3) : SWZ == 2 ? (blockIdx.x & ~31) + (blockIdx.x & 7) * 4 + ((blockIdx.x >> 3) & 3) : blockIdx.x) * BPW;
  const __amdgpu_buffer_rsrc_t rk = __builtin_amdgcn_make_buffer_rsrc((void*)(k + (size_t)bh0 * (SKV * DH)), 0, XBYTES * BPW, 0x00020000);
  const __amdgpu_buffer_rsrc_t rv = __builtin_amdgcn_make_buffer_rsrc((void*)(v + (size_t)bh0 * (SKV * DH)), 0, XBYTES * BPW, 0x00020000);
  const __amdgpu_buffer_rsrc_t re = __builtin_amdgcn_make_buffer_rsrc((void*)e, 0, PBYTES, 0x00020000);
  const __amdgpu_buffer_rsrc_t rf = __builtin_amdgcn_make_buffer_rsrc((void*)f, 0, PBYTES, 0x00020000);
  const int par = (wave ^ (blockIdx.x >> 3)) & 1;
  int xoff, poff, wr, rd;
  {
    const int lane = threadIdx.x & 63, g = lane >> 4, c = lane & 15;
    xoff = g * 2048 + c * 16;
    poff = (lane >> 3) * 8192 + (lane & 7) * 16;
    wr = wave * 16384 + (lane >> 3) * 256 + (((lane & 7) ^ (lane >> 3)) * 16);
    rd = wave * 16384 + c * 128 + ((g ^ (c >> 1) ^ (par * 4)) * 16);
  }
  const int rot = blockIdx.x * ROT;
  u32x4 X[8], R[4];
#define BLK(S) ((((((S) >> 1) + rot) & (ND - 1)) * NW + wave))
#define XS(S) (((S) / (2 * NS)) * XBYTES + BLK(S) * 16384 + ((((S) ^ par) & 1) * 8192))
#define LDX(rs, so, i) __builtin_amdgcn_raw_buffer_load_b128(rs, xoff + (i) * 256, so, XAUX)
#define PS(T) (((T) & 1) * (32 * 4096) + BLK(T) * 128 + (((T) >= 2 * BPW * NS) ? PBYTES : 0))
#define LDP(rs, so, i) __builtin_amdgcn_raw_buffer_load_b128(rs, poff, (so) + ((i) >> 1) * (16 * 4096) + ((i) & 1) * 4096, PAUX)
#define WRP(T, i, val) *reinterpret_cast<u32x4*>(smem + (wr + ((T) & 3) * 4096) + ((i) >> 1) * 2048 + ((i) & 1) * 128) = (val)
  {
    u32x4 R2[4];
#pragma unroll
    for (int i = 0; i < 8; ++i) X[i] = LDX(rk, XS(0), i);
    __builtin_amdgcn_sched_barrier(0);
#pragma unroll
    for (int i = 0; i < 4; ++i) R[i] = LDP(re, PS(0), i);
#pragma unroll
    for (int i = 0; i < 4; ++i) R2[i] = LDP(re, PS(1), i);
#pragma unroll
    for (int i = 0; i < 4; ++i) { WRP(0, i, R[i]); R[i] = LDP(re, PS(2), i); }
#pragma unroll
    for (int i = 0; i < 4; ++i) WRP(1, i, R2[i]);
  }
  f32x4 acc[4][8];
#pragma unroll 1
  for (int ph = 0; ph < 2 * BPW; ++ph) {
    const int b = ph >> 1;
    const bool isv = ph & 1;
#pragma unroll
    for (int t = 0; t < 4; ++t)
#pragma unroll
      for (int j = 0; j < 8; ++j) acc[t][j] = f32x4{0.f, 0.f, 0.f, 0.f};
#pragma unroll 1
    for (int S = ph * NS; S < (ph + 1) * NS; ++S) {
      const __amdgpu_buffer_rsrc_t rx = (((S + 1) / NS) & 1) ? rv : rk;
      const __amdgpu_buffer_rsrc_t rp = (((S + 3) / NS) & 1) ? rf : re;
      const int xs = XS(S + 1), ps = PS(S + 3);
      if ((S & 7) == 0) {
        if (S == 0) __builtin_amdgcn_s_setprio(3); else if (S == 8) __builtin_amdgcn_s_setprio(2); else if (S == 16) __builtin_amdgcn_s_setprio(1); else __builtin_amdgcn_s_setprio(0);
      }
      asm volatile("" : "+v"(xoff));
      u32x4 B[8], P[4];
      const char* rp_lds = smem + (rd ^ (((S & 1) << 6) | ((S & 2) << 12)));
#pragma unroll
      for (int t = 0; t < 4; ++t) P[t] = *reinterpret_cast<const u32x4*>(rp_lds + t * 2048);
      __builtin_amdgcn_sched_barrier(0);
#pragma unroll
      for (int u = 0; u < 4; ++u) {
#pragma unroll
        for (int d = 0; d < 4; ++d) {
          B[2 * d][u]     = __builtin_amdgcn_perm(X[2 * u + 1][d], X[2 * u][d], 0x05040100u);
          B[2 * d + 1][u] = __builtin_amdgcn_perm(X[2 * u + 1][d], X[2 * u][d], 0x07060302u);
        }
        __builtin_amdgcn_sched_barrier(0);
        X[2 * u] = LDX(rx, xs, 2 * u); X[2 * u + 1] = LDX(rx, xs, 2 * u + 1);
        __builtin_amdgcn_sched_barrier(0);
      }
#pragma unroll
      for (int t = 0; t < 4; ++t) {
        WRP(S + 2, t, R[t]);
        R[t] = LDP(rp, ps, t);
#pragma unroll
        for (int j = 0; j < 8; ++j)
          acc[t][j] = __builtin_amdgcn_mfma_f32_16x16x32_bf16(__builtin_bit_cast(bf16x8, P[t]), __builtin_bit_cast(bf16x8, B[j]), acc[t][j], 0, 0, 0);
        __builtin_amdgcn_sched_barrier(0);
      }
    }
    int tid = threadIdx.x;
    asm volatile("" : "+v"(tid));
    const int lane = tid & 63, g = lane >> 4, c = lane & 15;
    float* sc = lds + b * (NW * RK + NW * DH);
    float* op = sc + NW * RK;
    if (!isv) {
      const u32x4 qq = *reinterpret_cast<const u32x4*>(q + (size_t)(bh0 + b) * DH + 8 * c);
      float qf[8];
#pragma unroll
      for (int d = 0; d < 4; ++d) { qf[2 * d] = bf_lo(qq[d]); qf[2 * d + 1] = bf_hi(qq[d]); }
#pragma unroll
      for (int t = 0; t < 4; ++t) {
        f32x4 sv;
#pragma unroll
        for (int r = 0; r < 4; ++r) {
          float x = 0.f;
#pragma unroll
          for (int j = 0; j < 8; ++j) x = fmaf(qf[j], acc[t][j][r], x);
          sv[r] = row16_sum(x);
        }
        if (c == 0) *reinterpret_cast<f32x4*>(sc + wave * RK + 16 * t + 4 * g) = sv;
      }
    } else {
      __syncthreads();
      float s = 0.f;
#pragma unroll
      for (int w = 0; w < NW; ++w) s += sc[w * RK + lane];
      s *= 0.08838834764831845f * 1.4426950408889634f;
      const float m = xrow_max(row16_max(s));
      const float ex = __builtin_amdgcn_exp2f(s - m);
      const float sum = xrow_sum(row16_sum(ex));
      float* pw = lds + BPW * (NW * RK + NW * DH) + wave * RK;
      pw[lane] = ex / sum;
      __builtin_amdgcn_fence(__ATOMIC_RELEASE, "wavefront");
      __builtin_amdgcn_wave_barrier();
      __builtin_amdgcn_fence(__ATOMIC_ACQUIRE, "wavefront");
      float o[8];
#pragma unroll
      for (int j = 0; j < 8; ++j) o[j] = 0.f;
#pragma unroll
      for (int t = 0; t < 4; ++t) {
        const f32x4 pv = *reinterpret_cast<const f32x4*>(pw + 16 * t + 4 * g);
#pragma unroll
        for (int j = 0; j < 8; ++j)
#pragma unroll
          for (int r = 0; r < 4; ++r) o[j] = fmaf(pv[r], acc[t][j][r], o[j]);
      }
#pragma unroll
      for (int j = 0; j < 8; ++j) o[j] = xrow_sum(o[j]);
      if (g == 0) {
        *reinterpret_cast<f32x4*>(op + wave * DH + 8 * c) = f32x4{o[0], o[1], o[2], o[3]};
        *reinterpret_cast<f32x4*>(op + wave * DH + 8 * c + 4) = f32x4{o[4], o[5], o[6], o[7]};
      }
      __syncthreads();
      if (tid < DH) {
        float r = 0.f;
#pragma unroll
        for (int w = 0; w < NW; ++w) r += op[w * DH + tid];
        out[(size_t)(bh0 + b) * DH + tid] = __float2bfloat16(r);
      }
    }
  }
}
}

extern "C" void kernel_launch(void* const* d_in, const int* in_sizes, int n_in, void* d_out, int out_size, void* d_ws, size_t ws_size,
                              hipStream_t stream) {
  (void)in_sizes; (void)n_in; (void)out_size; (void)d_ws; (void)ws_size;
  lf::linf_kernel<LF_NW, LF_BPW, LF_ROT, 2, 0><<<lf::NBH / LF_BPW, LF_NW * 64, 0, stream>>>(
      (const unsigned short*)d_in[0], (const unsigned short*)d_in[1], (const unsigned short*)d_in[2],
      (const unsigned short*)d_in[3], (const unsigned short*)d_in[4], (__hip_bfloat16*)d_out);
}
```

```cpp
#include <hip/hip_runtime.h>
#include <hip/hip_bf16.h>
#include <stdint.h>

#define LF_NW 4
#define LF_BPW 1
#define LF_ROT 3

namespace lf {
typedef short bf16x8 __attribute__((ext_vector_type(8)));
typedef float f32x4 __attribute__((ext_vector_type(4)));
typedef unsigned u32x4 __attribute__((ext_vector_type(4)));

constexpr int NBH = 512, SKV = 2048, DH = 128, RK = 64;
constexpr unsigned XBYTES = SKV * DH * 2;
constexpr unsigned PBYTES = RK * SKV * 2;

template <int CTRL> __device__ __forceinline__ float dpp(float x) {
  return __builtin_bit_cast(float, __builtin_amdgcn_mov_dpp(__builtin_bit_cast(int, x), CTRL, 0xf, 0xf, true));
}
__device__ __forceinline__ float row16_sum(float x) {
  x += dpp<0x128>(x); x += dpp<0x124>(x); x += dpp<0x122>(x); x += dpp<0x121>(x);
  return x;
}
__device__ __forceinline__ float row16_max(float x) {
  x = fmaxf(x, dpp<0x128>(x)); x = fmaxf(x, dpp<0x124>(x)); x = fmaxf(x, dpp<0x122>(x)); x = fmaxf(x, dpp<0x121>(x));
  return x;
}
__device__ __forceinline__ float xrow_sum(float x) {
  auto s = __builtin_amdgcn_permlane16_swap(__float_as_uint(x), __float_as_uint(x), false, false);
  x = __uint_as_float(s[0]) + __uint_as_float(s[1]);
  auto t = __builtin_amdgcn_permlane32_swap(__float_as_uint(x), __float_as_uint(x), false, false);
  return __uint_as_float(t[0]) + __uint_as_float(t[1]);
}
__device__ __forceinline__ float xrow_max(float x) {
  auto s = __builtin_amdgcn_permlane16_swap(__float_as_uint(x), __float_as_uint(x), false, false);
  x = fmaxf(__uint_as_float(s[0]), __uint_as_float(s[1]));
  auto t = __builtin_amdgcn_permlane32_swap(__float_as_uint(x), __float_as_uint(x), false, false);
  return fmaxf(__uint_as_float(t[0]), __uint_as_float(t[1]));
}
__device__ __forceinline__ float bf_lo(unsigned u) { return __uint_as_float(u << 16); }
__device__ __forceinline__ float bf_hi(unsigned u) { return __uint_as_float(u & 0xffff0000u); }

template <int NW, int BPW, int ROT, int XAUX, int PAUX>
__global__ void __launch_bounds__(NW * 64, 2)
linf_kernel(const unsigned short* __restrict__ q, const unsigned short* __restrict__ k, const unsigned short* __restrict__ v,
            const unsigned short* __restrict__ e, const unsigned short* __restrict__ f, __hip_bfloat16* __restrict__ out) {
  constexpr int ND = SKV / (64 * NW);
  constexpr int NS = 2 * ND;
  __shared__ __attribute__((aligned(16))) char smem[NW * 16384 + (BPW * (NW * RK + NW * DH) + NW * RK) * 4];
  float* lds = reinterpret_cast<float*>(smem + NW * 16384);
  const int wave = __builtin_amdgcn_readfirstlane(threadIdx.x >> 6);
  const int bh0 = ((blockIdx.x & 7) * (NBH / BPW / 8) + (blockIdx.x >> 3)) * BPW;
  const __amdgpu_buffer_rsrc_t rk = __builtin_amdgcn_make_buffer_rsrc((void*)(k + (size_t)bh0 * (SKV * DH)), 0, XBYTES * BPW, 0x00020000);
  const __amdgpu_buffer_rsrc_t rv = __builtin_amdgcn_make_buffer_rsrc((void*)(v + (size_t)bh0 * (SKV * DH)), 0, XBYTES * BPW, 0x00020000);
  const __amdgpu_buffer_rsrc_t re = __builtin_amdgcn_make_buffer_rsrc((void*)e, 0, PBYTES, 0x00020000);
  const __amdgpu_buffer_rsrc_t rf = __builtin_amdgcn_make_buffer_rsrc((void*)f, 0, PBYTES, 0x00020000);
  const int par = (wave ^ (blockIdx.x >> 3)) & 1;
  int xoff, poff, wr, rd;
  {
    const int lane = threadIdx.x & 63, g = lane >> 4, c = lane & 15;
    xoff = g * 2048 + c * 16;
    poff = (lane >> 3) * 8192 + (lane & 7) * 16;
    wr = wave * 16384 + (lane >> 3) * 256 + (((lane & 7) ^ (lane >> 3)) * 16);
    rd = wave * 16384 + c * 128 + ((g ^ (c >> 1) ^ (par * 4)) * 16);
  }
  const int rot = blockIdx.x * ROT;
  u32x4 X[8], R[4];
#define BLK(S) ((((((S) >> 1) + rot) & (ND - 1)) * NW + wave))
#define XS(S) (((S) / (2 * NS)) * XBYTES + BLK(S) * 16384 + ((((S) ^ par) & 1) * 8192))
#define LDX(rs, so, i) __builtin_amdgcn_raw_buffer_load_b128(rs, xoff + (i) * 256, so, XAUX)
#define PS(T) (((T) & 1) * (32 * 4096) + BLK(T) * 128 + (((T) >= 2 * BPW * NS) ? PBYTES : 0))
#define LDP(rs, so, i) __builtin_amdgcn_raw_buffer_load_b128(rs, poff, (so) + ((i) >> 1) * (16 * 4096) + ((i) & 1) * 4096, PAUX)
#define WRP(T, i, val) *reinterpret_cast<u32x4*>(smem + (wr + ((T) & 3) * 4096) + ((i) >> 1) * 2048 + ((i) & 1) * 128) = (val)
  {
    u32x4 R2[4];
#pragma unroll
    for (int i = 0; i < 8; ++i) X[i] = LDX(rk, XS(0), i);
    __builtin_amdgcn_sched_barrier(0);
#pragma unroll
    for (int i = 0; i < 4; ++i) R[i] = LDP(re, PS(0), i);
#pragma unroll
    for (int i = 0; i < 4; ++i) R2[i] = LDP(re, PS(1), i);
#pragma unroll
    for (int i = 0; i < 4; ++i) { WRP(0, i, R[i]); R[i] = LDP(re, PS(2), i); }
#pragma unroll
    for (int i = 0; i < 4; ++i) WRP(1, i, R2[i]);
  }
  f32x4 acc[4][8];
#pragma unroll 1
  for (int ph = 0; ph < 2 * BPW; ++ph) {
    const int b = ph >> 1;
    const bool isv = ph & 1;
#pragma unroll
    for (int t = 0; t < 4; ++t)
#pragma unroll
      for (int j = 0; j < 8; ++j) acc[t][j] = f32x4{0.f, 0.f, 0.f, 0.f};
#pragma unroll 1
    for (int S = ph * NS; S < (ph + 1) * NS; ++S) {
      const __amdgpu_buffer_rsrc_t rx = (((S + 1) / NS) & 1) ? rv : rk;
      const __amdgpu_buffer_rsrc_t rp = (((S + 3) / NS) & 1) ? rf : re;
      const int xs = XS(S + 1), ps = PS(S + 3);
      if ((S & 7) == 0) {
        if (S == 0) __builtin_amdgcn_s_setprio(3);
        else if (S == 8) __builtin_amdgcn_s_setprio(2);
        else if (S == 16) __builtin_amdgcn_s_setprio(1);
        else __builtin_amdgcn_s_setprio(0);
      }
      asm volatile("" : "+v"(xoff));
      u32x4 B[8], P[4];
      const char* rp_lds = smem + (rd ^ (((S & 1) << 6) | ((S & 2) << 12)));
#pragma unroll
      for (int t = 0; t < 4; ++t) P[t] = *reinterpret_cast<const u32x4*>(rp_lds + t * 2048);
      __builtin_amdgcn_sched_barrier(0);
#pragma unroll
      for (int u = 0; u < 4; ++u) {
#pragma unroll
        for (int d = 0; d < 4; ++d) {
          B[2 * d][u]     = __builtin_amdgcn_perm(X[2 * u + 1][d], X[2 * u][d], 0x05040100u);
          B[2 * d + 1][u] = __builtin_amdgcn_perm(X[2 * u + 1][d], X[2 * u][d], 0x07060302u);
        }
        __builtin_amdgcn_sched_barrier(0);
        X[2 * u] = LDX(rx, xs, 2 * u); X[2 * u + 1] = LDX(rx, xs, 2 * u + 1);
        __builtin_amdgcn_sched_barrier(0);
      }
#pragma unroll
      for (int t = 0; t < 4; ++t) {
        WRP(S + 2, t, R[t]);
        R[t] = LDP(rp, ps, t);
#pragma unroll
        for (int j = 0; j < 8; ++j)
          acc[t][j] = __builtin_amdgcn_mfma_f32_16x16x32_bf16(__builtin_bit_cast(bf16x8, P[t]), __builtin_bit_cast(bf16x8, B[j]), acc[t][j], 0, 0, 0);
        __builtin_amdgcn_sched_barrier(0);
      }
    }
    int tid = threadIdx.x;
    asm volatile("" : "+v"(tid));
    const int lane = tid & 63, g = lane >> 4, c = lane & 15;
    float* sc = lds + b * (NW * RK + NW * DH);
    float* op = sc + NW * RK;
    if (!isv) {
      const u32x4 qq = *reinterpret_cast<const u32x4*>(q + (size_t)(bh0 + b) * DH + 8 * c);
      float qf[8];
#pragma unroll
      for (int d = 0; d < 4; ++d) { qf[2 * d] = bf_lo(qq[d]); qf[2 * d + 1] = bf_hi(qq[d]); }
#pragma unroll
      for (int t = 0; t < 4; ++t) {
        f32x4 sv;
#pragma unroll
        for (int r = 0; r < 4; ++r) {
          float x = 0.f;
#pragma unroll
          for (int j = 0; j < 8; ++j) x = fmaf(qf[j], acc[t][j][r], x);
          sv[r] = row16_sum(x);
        }
        if (c == 0) *reinterpret_cast<f32x4*>(sc + wave * RK + 16 * t + 4 * g) = sv;
      }
    } else {
      __syncthreads();
      float s = 0.f;
#pragma unroll
      for (int w = 0; w < NW; ++w) s += sc[w * RK + lane];
      s *= 0.08838834764831845f * 1.4426950408889634f;
      const float m = xrow_max(row16_max(s));
      const float ex = __builtin_amdgcn_exp2f(s - m);
      const float sum = xrow_sum(row16_sum(ex));
      float* pw = lds + BPW * (NW * RK + NW * DH) + wave * RK;
      pw[lane] = ex / sum;
      __builtin_amdgcn_fence(__ATOMIC_RELEASE, "wavefront");
      __builtin_amdgcn_wave_barrier();
      __builtin_amdgcn_fence(__ATOMIC_ACQUIRE, "wavefront");
      float o[8];
#pragma unroll
      for (int j = 0; j < 8; ++j) o[j] = 0.f;
#pragma unroll
      for (int t = 0; t < 4; ++t) {
        const f32x4 pv = *reinterpret_cast<const f32x4*>(pw + 16 * t + 4 * g);
#pragma unroll
        for (int j = 0; j < 8; ++j)
#pragma unroll
          for (int r = 0; r < 4; ++r) o[j] = fmaf(pv[r], acc[t][j][r], o[j]);
      }
#pragma unroll
      for (int j = 0; j < 8; ++j) o[j] = xrow_sum(o[j]);
      if (g == 0) {
        *reinterpret_cast<f32x4*>(op + wave * DH + 8 * c) = f32x4{o[0], o[1], o[2], o[3]};
        *reinterpret_cast<f32x4*>(op + wave * DH + 8 * c + 4) = f32x4{o[4], o[5], o[6], o[7]};
      }
      __syncthreads();
      if (tid < DH) {
        float r = 0.f;
#pragma unroll
        for (int w = 0; w < NW; ++w) r += op[w * DH + tid];
        out[(size_t)(bh0 + b) * DH + tid] = __float2bfloat16(r);
      }
    }
  }
}
}

extern "C" void kernel_launch(void* const* d_in, const int* in_sizes, int n_in, void* d_out, int out_size, void* d_ws, size_t ws_size,
                              hipStream_t stream) {
  (void)in_sizes; (void)n_in; (void)out_size; (void)d_ws; (void)ws_size;
  lf::linf_kernel<LF_NW, LF_BPW, LF_ROT, 2, 0><<<lf::NBH / LF_BPW, LF_NW * 64, 0, stream>>>(
      (const unsigned short*)d_in[0], (const unsigned short*)d_in[1], (const unsigned short*)d_in[2],
      (const unsigned short*)d_in[3], (const unsigned short*)d_in[4], (__hip_bfloat16*)d_out);
}
```

```cpp
#include <hip/hip_runtime.h>
#include <hip/hip_bf16.h>
#include <stdint.h>

namespace lf {
typedef short bf16x8 __attribute__((ext_vector_type(8)));
typedef float f32x4 __attribute__((ext_vector_type(4)));
typedef unsigned u32x4 __attribute__((ext_vector_type(4)));

constexpr int NBH = 512, SKV = 2048, DH = 128, RK = 64;
constexpr unsigned XBYTES = SKV * DH * 2;
constexpr unsigned PBYTES = RK * SKV * 2;

template <int CTRL> __device__ __forceinline__ float dpp(float x) {
  return __builtin_bit_cast(float, __builtin_amdgcn_mov_dpp(__builtin_bit_cast(int, x), CTRL, 0xf, 0xf, true));
}
__device__ __forceinline__ float row16_sum(float x) {
  x += dpp<0x128>(x); x += dpp<0x124>(x); x += dpp<0x122>(x); x += dpp<0x121>(x);
  return x;
}
__device__ __forceinline__ float row16_max(float x) {
  x = fmaxf(x, dpp<0x128>(x)); x = fmaxf(x, dpp<0x124>(x)); x = fmaxf(x, dpp<0x122>(x)); x = fmaxf(x, dpp<0x121>(x));
  return x;
}
__device__ __forceinline__ float xrow_sum(float x) {
  auto s = __builtin_amdgcn_permlane16_swap(__float_as_uint(x), __float_as_uint(x), false, false);
  x = __uint_as_float(s[0]) + __uint_as_float(s[1]);
  auto t = __builtin_amdgcn_permlane32_swap(__float_as_uint(x), __float_as_uint(x), false, false);
  return __uint_as_float(t[0]) + __uint_as_float(t[1]);
}
__device__ __forceinline__ float xrow_max(float x) {
  auto s = __builtin_amdgcn_permlane16_swap(__float_as_uint(x), __float_as_uint(x), false, false);
  x = fmaxf(__uint_as_float(s[0]), __uint_as_float(s[1]));
  auto t = __builtin_amdgcn_permlane32_swap(__float_as_uint(x), __float_as_uint(x), false, false);
  return fmaxf(__uint_as_float(t[0]), __uint_as_float(t[1]));
}
__device__ __forceinline__ float bf_lo(unsigned u) { return __uint_as_float(u << 16); }
__device__ __forceinline__ float bf_hi(unsigned u) { return __uint_as_float(u & 0xffff0000u); }

__global__ void __launch_bounds__(256, 2)
linf_kernel(const unsigned short* __restrict__ q, const unsigned short* __restrict__ k, const unsigned short* __restrict__ v,
     const unsigned short* __restrict__ e, const unsigned short* __restrict__ f, __hip_bfloat16* __restrict__ out) {
  constexpr int NW = 4, BPW = 2, XAUX = 2, PAUX = 0, ROT = 3, SM = 2;
  constexpr int ND = SKV / (64 * NW);
  constexpr int NS = 2 * ND;
  __shared__ __attribute__((aligned(16))) char smem[NW * 16384 + (BPW * NW * RK + BPW * NW * DH + NW * RK) * 4];
  float* lds = reinterpret_cast<float*>(smem + NW * 16384);
  const int wave = __builtin_amdgcn_readfirstlane(threadIdx.x >> 6);
  const int bh0 = ((blockIdx.x & 7) * (NBH / BPW / 8) + (blockIdx.x >> 3)) * BPW;
  const __amdgpu_buffer_rsrc_t rk = __builtin_amdgcn_make_buffer_rsrc((void*)(k + (size_t)bh0 * (SKV * DH)), 0, XBYTES * BPW, 0x00020000);
  const __amdgpu_buffer_rsrc_t rv = __builtin_amdgcn_make_buffer_rsrc((void*)(v + (size_t)bh0 * (SKV * DH)), 0, XBYTES * BPW, 0x00020000);
  const __amdgpu_buffer_rsrc_t re = __builtin_amdgcn_make_buffer_rsrc((void*)e, 0, PBYTES, 0x00020000);
  const __amdgpu_buffer_rsrc_t rf = __builtin_amdgcn_make_buffer_rsrc((void*)f, 0, PBYTES, 0x00020000);
  const int par = (wave ^ (blockIdx.x >> 3)) & 1;
  int xoff, poff, wr, rd;
  u32x4 qq[2];
  {
    const int lane = threadIdx.x & 63, g = lane >> 4, c = lane & 15;
    xoff = g * 2048 + c * 16;
    poff = (lane >> 3) * 8192 + (lane & 7) * 16;
    wr = wave * 16384 + (lane >> 3) * 256 + (((lane & 7) ^ (lane >> 3)) * 16);
    rd = wave * 16384 + c * 128 + ((g ^ (c >> 1) ^ (par * 4)) * 16);
  }
  const int rot = blockIdx.x * ROT;
  u32x4 X[8], R[4];
#define BLK(S) ((((((S) >> 1) + rot) & (ND - 1)) * NW + wave))
#define XS(S) (((S) / (2 * NS)) * XBYTES + BLK(S) * 16384 + ((((S) ^ par) & 1) * 8192))
#define LDX(rs, so, i) __builtin_amdgcn_raw_buffer_load_b128(rs, xoff + (i) * 256, so, XAUX)
#define PS(T) (((T) & 1) * (32 * 4096) + BLK(T) * 128 + (((T) >= 2 * BPW * NS) ? PBYTES : 0))
#define LDP(rs, so, i) __builtin_amdgcn_raw_buffer_load_b128(rs, poff, (so) + ((i) >> 1) * (16 * 4096) + ((i) & 1) * 4096, PAUX)
#define WRP(T, i, val) *reinterpret_cast<u32x4*>(smem + (wr + ((T) & 3) * 4096) + ((i) >> 1) * 2048 + ((i) & 1) * 128) = (val)
  {
    u32x4 R2[4];
#pragma unroll
    for (int i = 0; i < 8; ++i) X[i] = LDX(rk, XS(0), i);
    __builtin_amdgcn_sched_barrier(0);
#pragma unroll
    for (int i = 0; i < 4; ++i) R[i] = LDP(re, PS(0), i);
#pragma unroll
    for (int i = 0; i < 4; ++i) R2[i] = LDP(re, PS(1), i);
    qq[0] = *reinterpret_cast<const u32x4*>(q + (size_t)bh0 * DH + 8 * (threadIdx.x & 15));
    qq[1] = *reinterpret_cast<const u32x4*>(q + (size_t)(bh0 + 1) * DH + 8 * (threadIdx.x & 15));
    __builtin_amdgcn_sched_barrier(0);
#pragma unroll
    for (int i = 0; i < 4; ++i) { WRP(0, i, R[i]); R[i] = LDP(re, PS(2), i); }
#pragma unroll
    for (int i = 0; i < 4; ++i) WRP(1, i, R2[i]);
    asm volatile("" : "+v"(qq[0]), "+v"(qq[1]));
  }
  f32x4 acc[4][8];
#pragma unroll
  for (int t = 0; t < 4; ++t)
#pragma unroll
    for (int j = 0; j < 8; ++j) acc[t][j] = f32x4{0.f, 0.f, 0.f, 0.f};
  int S = 0;
#pragma unroll 1
  for (int seg = 0; seg < 3 * BPW; ++seg) {
    const int b = seg >= 3, ty = seg - 3 * b;
    const int send = b * 2 * NS + (ty == 0 ? NS : ty == 1 ? NS + SM : 2 * NS);
#pragma unroll 1
    for (; S < send; ++S) {
      const __amdgpu_buffer_rsrc_t rx = (((S + 1) / NS) & 1) ? rv : rk;
      const __amdgpu_buffer_rsrc_t rp = (((S + 3) / NS) & 1) ? rf : re;
      const int xs = XS(S + 1), ps = PS(S + 3);
      asm volatile("" : "+v"(xoff));
      u32x4 B[8], P[4];
      const char* rp_lds = smem + (rd ^ (((S & 1) << 6) | ((S & 2) << 12)));
#pragma unroll
      for (int t = 0; t < 4; ++t) P[t] = *reinterpret_cast<const u32x4*>(rp_lds + t * 2048);
      __builtin_amdgcn_sched_barrier(0);
#pragma unroll
      for (int u = 0; u < 4; ++u) {
#pragma unroll
        for (int d = 0; d < 4; ++d) {
          B[2 * d][u]     = __builtin_amdgcn_perm(X[2 * u + 1][d], X[2 * u][d], 0x05040100u);
          B[2 * d + 1][u] = __builtin_amdgcn_perm(X[2 * u + 1][d], X[2 * u][d], 0x07060302u);
        }
        __builtin_amdgcn_sched_barrier(0);
        X[2 * u] = LDX(rx, xs, 2 * u); X[2 * u + 1] = LDX(rx, xs, 2 * u + 1);
        __builtin_amdgcn_sched_barrier(0);
      }
#pragma unroll
      for (int t = 0; t < 4; ++t) {
        WRP(S + 2, t, R[t]);
        R[t] = LDP(rp, ps, t);
#pragma unroll
        for (int j = 0; j < 8; ++j)
          acc[t][j] = __builtin_amdgcn_mfma_f32_16x16x32_bf16(__builtin_bit_cast(bf16x8, P[t]), __builtin_bit_cast(bf16x8, B[j]), acc[t][j], 0, 0, 0);
        __builtin_amdgcn_sched_barrier(0);
      }
    }
    int tid = threadIdx.x;
    asm volatile("" : "+v"(tid));
    const int lane = tid & 63, g = lane >> 4, c = lane & 15;
    float* sc = lds + b * (NW * RK);
    float* pw = lds + 2 * NW * RK + wave * RK;
    float* op = lds + 3 * NW * RK + b * (NW * DH);
    if (ty == 1) {
      __builtin_amdgcn_fence(__ATOMIC_RELEASE, "workgroup", "local");
      __builtin_amdgcn_s_barrier();
      __builtin_amdgcn_fence(__ATOMIC_ACQUIRE, "workgroup", "local");
      float s = 0.f;
#pragma unroll
      for (int w = 0; w < NW; ++w) s += sc[w * RK + lane];
      s *= 0.08838834764831845f * 1.4426950408889634f;
      const float m = xrow_max(row16_max(s));
      const float ex = __builtin_amdgcn_exp2f(s - m);
      const float sum = xrow_sum(row16_sum(ex));
      pw[lane] = ex / sum;
    } else {
      if (ty == 0) {
        const u32x4 qv = b ? qq[1] : qq[0];
        float qf[8];
#pragma unroll
        for (int d = 0; d < 4; ++d) { qf[2 * d] = bf_lo(qv[d]); qf[2 * d + 1] = bf_hi(qv[d]); }
#pragma unroll
        for (int t = 0; t < 4; ++t) {
          f32x4 sv;
#pragma unroll
          for (int r = 0; r < 4; ++r) {
            float x = 0.f;
#pragma unroll
            for (int j = 0; j < 8; ++j) x = fmaf(qf[j], acc[t][j][r], x);
            sv[r] = row16_sum(x);
          }
          if (c == 0) *reinterpret_cast<f32x4*>(sc + wave * RK + 16 * t + 4 * g) = sv;
        }
      } else {
        __builtin_amdgcn_fence(__ATOMIC_RELEASE, "wavefront", "local");
        __builtin_amdgcn_wave_barrier();
        __builtin_amdgcn_fence(__ATOMIC_ACQUIRE, "wavefront", "local");
        float o[8];
#pragma unroll
        for (int j = 0; j < 8; ++j) o[j] = 0.f;
#pragma unroll
        for (int t = 0; t < 4; ++t) {
          const f32x4 pv = *reinterpret_cast<const f32x4*>(pw + 16 * t + 4 * g);
#pragma unroll
          for (int j = 0; j < 8; ++j)
#pragma unroll
            for (int r = 0; r < 4; ++r) o[j] = fmaf(pv[r], acc[t][j][r], o[j]);
        }
#pragma unroll
        for (int j = 0; j < 8; ++j) o[j] = xrow_sum(o[j]);
        if (g == 0) {
          *reinterpret_cast<f32x4*>(op + wave * DH + 8 * c) = f32x4{o[0], o[1], o[2], o[3]};
          *reinterpret_cast<f32x4*>(op + wave * DH + 8 * c + 4) = f32x4{o[4], o[5], o[6], o[7]};
        }
      }
#pragma unroll
      for (int t = 0; t < 4; ++t)
#pragma unroll
        for (int j = 0; j < 8; ++j) acc[t][j] = f32x4{0.f, 0.f, 0.f, 0.f};
    }
  }
  __builtin_amdgcn_fence(__ATOMIC_RELEASE, "workgroup", "local");
  __builtin_amdgcn_s_barrier();
  __builtin_amdgcn_fence(__ATOMIC_ACQUIRE, "workgroup", "local");
  {
    const int tid = threadIdx.x;
    const float* op = lds + 3 * NW * RK + (tid >> 7) * (NW * DH) + (tid & 127);
    float r = 0.f;
#pragma unroll
    for (int w = 0; w < NW; ++w) r += op[w * DH];
    out[(size_t)bh0 * DH + tid] = __float2bfloat16(r);
  }
}
}

extern "C" void kernel_launch(void* const* d_in, const int* in_sizes, int n_in, void* d_out, int out_size, void* d_ws, size_t ws_size,
                              hipStream_t stream) {
  (void)in_sizes; (void)n_in; (void)out_size; (void)d_ws; (void)ws_size;
  lf::linf_kernel<<<lf::NBH / 2, 256, 16384, stream>>>(
      (const unsigned short*)d_in[0], (const unsigned short*)d_in[1], (const unsigned short*)d_in[2],
      (const unsigned short*)d_in[3], (const unsigned short*)d_in[4], (__hip_bfloat16*)d_out);
}
```

```cpp
#define MEAS 0
#define SEL (blockIdx.x == 100 && wave == 1)
#include <hip/hip_runtime.h>
#include <hip/hip_bf16.h>
#include <stdint.h>

namespace lf {
typedef short bf16x8 __attribute__((ext_vector_type(8)));
typedef float f32x4 __attribute__((ext_vector_type(4)));
typedef unsigned u32x4 __attribute__((ext_vector_type(4)));

constexpr int NBH = 512, SKV = 2048, DH = 128, RK = 64;
constexpr unsigned XBYTES = SKV * DH * 2;
constexpr unsigned PBYTES = RK * SKV * 2;

template <int CTRL> __device__ __forceinline__ float dpp(float x) {
  return __builtin_bit_cast(float, __builtin_amdgcn_mov_dpp(__builtin_bit_cast(int, x), CTRL, 0xf, 0xf, true));
}
__device__ __forceinline__ float row16_sum(float x) {
  x += dpp<0x128>(x); x += dpp<0x124>(x); x += dpp<0x122>(x); x += dpp<0x121>(x);
  return x;
}
__device__ __forceinline__ float row16_max(float x) {
  x = fmaxf(x, dpp<0x128>(x)); x = fmaxf(x, dpp<0x124>(x)); x = fmaxf(x, dpp<0x122>(x)); x = fmaxf(x, dpp<0x121>(x));
  return x;
}
__device__ __forceinline__ float xrow_sum(float x) {
  auto s = __builtin_amdgcn_permlane16_swap(__float_as_uint(x), __float_as_uint(x), false, false);
  x = __uint_as_float(s[0]) + __uint_as_float(s[1]);
  auto t = __builtin_amdgcn_permlane32_swap(__float_as_uint(x), __float_as_uint(x), false, false);
  return __uint_as_float(t[0]) + __uint_as_float(t[1]);
}
__device__ __forceinline__ float xrow_max(float x) {
  auto s = __builtin_amdgcn_permlane16_swap(__float_as_uint(x), __float_as_uint(x), false, false);
  x = fmaxf(__uint_as_float(s[0]), __uint_as_float(s[1]));
  auto t = __builtin_amdgcn_permlane32_swap(__float_as_uint(x), __float_as_uint(x), false, false);
  return fmaxf(__uint_as_float(t[0]), __uint_as_float(t[1]));
}
__device__ __forceinline__ float bf_lo(unsigned u) { return __uint_as_float(u << 16); }
__device__ __forceinline__ float bf_hi(unsigned u) { return __uint_as_float(u & 0xffff0000u); }

__global__ void __launch_bounds__(256, 2)
linf_kernel(const unsigned short* __restrict__ q, const unsigned short* __restrict__ k, const unsigned short* __restrict__ v,
     const unsigned short* __restrict__ e, const unsigned short* __restrict__ f, __hip_bfloat16* __restrict__ out) {
  constexpr int NW = 4, BPW = 2, XAUX = 2, PAUX = 0, ROT = 3, SM = 2;
  const unsigned long long t0 = __builtin_amdgcn_s_memrealtime();
  unsigned long long ta = t0, tb = t0;
  constexpr int ND = SKV / (64 * NW);
  constexpr int NS = 2 * ND;
  __shared__ __attribute__((aligned(16))) char smem[NW * 16384 + (BPW * NW * RK + BPW * NW * DH + NW * RK) * 4];
  float* lds = reinterpret_cast<float*>(smem + NW * 16384);
  const int wave = __builtin_amdgcn_readfirstlane(threadIdx.x >> 6);
  const int bh0 = ((blockIdx.x & 7) * (NBH / BPW / 8) + (blockIdx.x >> 3)) * BPW;
  const __amdgpu_buffer_rsrc_t rk = __builtin_amdgcn_make_buffer_rsrc((void*)(k + (size_t)bh0 * (SKV * DH)), 0, XBYTES * BPW, 0x00020000);
  const __amdgpu_buffer_rsrc_t rv = __builtin_amdgcn_make_buffer_rsrc((void*)(v + (size_t)bh0 * (SKV * DH)), 0, XBYTES * BPW, 0x00020000);
  const __amdgpu_buffer_rsrc_t re = __builtin_amdgcn_make_buffer_rsrc((void*)e, 0, PBYTES, 0x00020000);
  const __amdgpu_buffer_rsrc_t rf = __builtin_amdgcn_make_buffer_rsrc((void*)f, 0, PBYTES, 0x00020000);
  const int par = (wave ^ (blockIdx.x >> 3)) & 1;
  int xoff, poff, wr, rd;
  u32x4 qq[2];
  {
    const int lane = threadIdx.x & 63, g = lane >> 4, c = lane & 15;
    xoff = g * 2048 + c * 16;
    poff = (lane >> 3) * 8192 + (lane & 7) * 16;
    wr = wave * 16384 + (lane >> 3) * 256 + (((lane & 7) ^ (lane >> 3)) * 16);
    rd = wave * 16384 + c * 128 + ((g ^ (c >> 1) ^ (par * 4)) * 16);
  }
  const int rot = blockIdx.x * ROT;
  u32x4 X[8], R[4];
#define BLK(S) ((((((S) >> 1) + rot) & (ND - 1)) * NW + wave))
#define XS(S) (((S) / (2 * NS)) * XBYTES + BLK(S) * 16384 + ((((S) ^ par) & 1) * 8192))
#define LDX(rs, so, i) __builtin_amdgcn_raw_buffer_load_b128(rs, xoff + (i) * 256, so, XAUX)
#define PS(T) (((T) & 1) * (32 * 4096) + BLK(T) * 128 + (((T) >= 2 * BPW * NS) ? PBYTES : 0))
#define LDP(rs, so, i) __builtin_amdgcn_raw_buffer_load_b128(rs, poff, (so) + ((i) >> 1) * (16 * 4096) + ((i) & 1) * 4096, PAUX)
#define WRP(T, i, val) *reinterpret_cast<u32x4*>(smem + (wr + ((T) & 3) * 4096) + ((i) >> 1) * 2048 + ((i) & 1) * 128) = (val)
  {
    u32x4 R2[4];
#pragma unroll
    for (int i = 0; i < 8; ++i) X[i] = LDX(rk, XS(0), i);
    __builtin_amdgcn_sched_barrier(0);
#pragma unroll
    for (int i = 0; i < 4; ++i) R[i] = LDP(re, PS(0), i);
#pragma unroll
    for (int i = 0; i < 4; ++i) R2[i] = LDP(re, PS(1), i);
    qq[0] = *reinterpret_cast<const u32x4*>(q + (size_t)bh0 * DH + 8 * (threadIdx.x & 15));
    qq[1] = *reinterpret_cast<const u32x4*>(q + (size_t)(bh0 + 1) * DH + 8 * (threadIdx.x & 15));
    __builtin_amdgcn_sched_barrier(0);
#pragma unroll
    for (int i = 0; i < 4; ++i) { WRP(0, i, R[i]); R[i] = LDP(re, PS(2), i); }
#pragma unroll
    for (int i = 0; i < 4; ++i) WRP(1, i, R2[i]);
    asm volatile("" : "+v"(qq[0]), "+v"(qq[1]));
  }
  __builtin_amdgcn_sched_barrier(0);
  if (MEAS == 1) tb = __builtin_amdgcn_s_memrealtime();
  if (MEAS == 2) ta = __builtin_amdgcn_s_memrealtime();
  __builtin_amdgcn_sched_barrier(0);
  f32x4 acc[4][8];
#pragma unroll
  for (int t = 0; t < 4; ++t)
#pragma unroll
    for (int j = 0; j < 8; ++j) acc[t][j] = f32x4{0.f, 0.f, 0.f, 0.f};
  int S = 0;
#pragma unroll 1
  for (int seg = 0; seg < 3 * BPW; ++seg) {
    const int b = seg >= 3, ty = seg - 3 * b;
    const int send = b * 2 * NS + (ty == 0 ? NS : ty == 1 ? NS + SM : 2 * NS);
#pragma unroll 1
    for (; S < send; ++S) {
      const __amdgpu_buffer_rsrc_t rx = (((S + 1) / NS) & 1) ? rv : rk;
      const __amdgpu_buffer_rsrc_t rp = (((S + 3) / NS) & 1) ? rf : re;
      const int xs = XS(S + 1), ps = PS(S + 3);
      asm volatile("" : "+v"(xoff));
      if (MEAS == 2 && S == 1) tb = __builtin_amdgcn_s_memrealtime();
      u32x4 B[8], P[4];
      const char* rp_lds = smem + (rd ^ (((S & 1) << 6) | ((S & 2) << 12)));
#pragma unroll
      for (int t = 0; t < 4; ++t) P[t] = *reinterpret_cast<const u32x4*>(rp_lds + t * 2048);
      __builtin_amdgcn_sched_barrier(0);
#pragma unroll
      for (int u = 0; u < 4; ++u) {
#pragma unroll
        for (int d = 0; d < 4; ++d) {
          B[2 * d][u]     = __builtin_amdgcn_perm(X[2 * u + 1][d], X[2 * u][d], 0x05040100u);
          B[2 * d + 1][u] = __builtin_amdgcn_perm(X[2 * u + 1][d], X[2 * u][d], 0x07060302u);
        }
        __builtin_amdgcn_sched_barrier(0);
        X[2 * u] = LDX(rx, xs, 2 * u); X[2 * u + 1] = LDX(rx, xs, 2 * u + 1);
        __builtin_amdgcn_sched_barrier(0);
      }
#pragma unroll
      for (int t = 0; t < 4; ++t) {
        WRP(S + 2, t, R[t]);
        R[t] = LDP(rp, ps, t);
#pragma unroll
        for (int j = 0; j < 8; ++j)
          acc[t][j] = __builtin_amdgcn_mfma_f32_16x16x32_bf16(__builtin_bit_cast(bf16x8, P[t]), __builtin_bit_cast(bf16x8, B[j]), acc[t][j], 0, 0, 0);
        __builtin_amdgcn_sched_barrier(0);
      }
    }
    __builtin_amdgcn_sched_barrier(0);
    if (MEAS >= 3 && seg == MEAS - 3) ta = __builtin_amdgcn_s_memrealtime();
    __builtin_amdgcn_sched_barrier(0);
    int tid = threadIdx.x;
    asm volatile("" : "+v"(tid));
    const int lane = tid & 63, g = lane >> 4, c = lane & 15;
    float* sc = lds + b * (NW * RK);
    float* pw = lds + 2 * NW * RK + wave * RK;
    float* op = lds + 3 * NW * RK + b * (NW * DH);
    if (ty == 1) {
      __builtin_amdgcn_fence(__ATOMIC_RELEASE, "workgroup", "local");
      __builtin_amdgcn_s_barrier();
      __builtin_amdgcn_fence(__ATOMIC_ACQUIRE, "workgroup", "local");
      float s = 0.f;
#pragma unroll
      for (int w = 0; w < NW; ++w) s += sc[w * RK + lane];
      s *= 0.08838834764831845f * 1.4426950408889634f;
      const float m = xrow_max(row16_max(s));
      const float ex = __builtin_amdgcn_exp2f(s - m);
      const float sum = xrow_sum(row16_sum(ex));
      pw[lane] = ex / sum;
    } else {
      if (ty == 0) {
        const u32x4 qv = b ? qq[1] : qq[0];
        float qf[8];
#pragma unroll
        for (int d = 0; d < 4; ++d) { qf[2 * d] = bf_lo(qv[d]); qf[2 * d + 1] = bf_hi(qv[d]); }
#pragma unroll
        for (int t = 0; t < 4; ++t) {
          f32x4 sv;
#pragma unroll
          for (int r = 0; r < 4; ++r) {
            float x = 0.f;
#pragma unroll
            for (int j = 0; j < 8; ++j) x = fmaf(qf[j], acc[t][j][r], x);
            sv[r] = row16_sum(x);
          }
          if (c == 0) *reinterpret_cast<f32x4*>(sc + wave * RK + 16 * t + 4 * g) = sv;
        }
      } else {
        __builtin_amdgcn_fence(__ATOMIC_RELEASE, "wavefront", "local");
        __builtin_amdgcn_wave_barrier();
        __builtin_amdgcn_fence(__ATOMIC_ACQUIRE, "wavefront", "local");
        float o[8];
#pragma unroll
        for (int j = 0; j < 8; ++j) o[j] = 0.f;
#pragma unroll
        for (int t = 0; t < 4; ++t) {
          const f32x4 pv = *reinterpret_cast<const f32x4*>(pw + 16 * t + 4 * g);
#pragma unroll
          for (int j = 0; j < 8; ++j)
#pragma unroll
            for (int r = 0; r < 4; ++r) o[j] = fmaf(pv[r], acc[t][j][r], o[j]);
        }
#pragma unroll
        for (int j = 0; j < 8; ++j) o[j] = xrow_sum(o[j]);
        if (g == 0) {
          *reinterpret_cast<f32x4*>(op + wave * DH + 8 * c) = f32x4{o[0], o[1], o[2], o[3]};
          *reinterpret_cast<f32x4*>(op + wave * DH + 8 * c + 4) = f32x4{o[4], o[5], o[6], o[7]};
        }
      }
#pragma unroll
      for (int t = 0; t < 4; ++t)
#pragma unroll
        for (int j = 0; j < 8; ++j) acc[t][j] = f32x4{0.f, 0.f, 0.f, 0.f};
    }
    __builtin_amdgcn_sched_barrier(0);
    if (MEAS >= 3 && seg == MEAS - 3) tb = __builtin_amdgcn_s_memrealtime();
    __builtin_amdgcn_sched_barrier(0);
  }
  __builtin_amdgcn_fence(__ATOMIC_RELEASE, "workgroup", "local");
  __builtin_amdgcn_s_barrier();
  __builtin_amdgcn_fence(__ATOMIC_ACQUIRE, "workgroup", "local");
  {
    const int tid = threadIdx.x;
    const float* op = lds + 3 * NW * RK + (tid >> 7) * (NW * DH) + (tid & 127);
    float r = 0.f;
#pragma unroll
    for (int w = 0; w < NW; ++w) r += op[w * DH];
    out[(size_t)bh0 * DH + tid] = __float2bfloat16(r);
  }
  const unsigned long long tt = t0 + 12000 + 10 * (tb - ta);
  if (SEL) while (__builtin_amdgcn_s_memrealtime() < tt) __builtin_amdgcn_s_sleep(4);
}
}

extern "C" void kernel_launch(void* const* d_in, const int* in_sizes, int n_in, void* d_out, int out_size, void* d_ws, size_t ws_size,
                              hipStream_t stream) {
  (void)in_sizes; (void)n_in; (void)out_size; (void)d_ws; (void)ws_size;
  lf::linf_kernel<<<lf::NBH / 2, 256, 0, stream>>>(
      (const unsigned short*)d_in[0], (const unsigned short*)d_in[1], (const unsigned short*)d_in[2],
      (const unsigned short*)d_in[3], (const unsigned short*)d_in[4], (__hip_bfloat16*)d_out);
}
```

```cpp
#include <hip/hip_runtime.h>
#include <hip/hip_bf16.h>
#include <stdint.h>

namespace lf {
typedef short bf16x8 __attribute__((ext_vector_type(8)));
typedef float f32x4 __attribute__((ext_vector_type(4)));
typedef float f32x2 __attribute__((ext_vector_type(2)));
typedef unsigned u32x4 __attribute__((ext_vector_type(4)));

constexpr int NBH = 512, SKV = 2048, DH = 128, RK = 64;
constexpr unsigned XBYTES = SKV * DH * 2;
constexpr unsigned PBYTES = RK * SKV * 2;

template <int CTRL> __device__ __forceinline__ float dpp(float x) {
  return __builtin_bit_cast(float, __builtin_amdgcn_mov_dpp(__builtin_bit_cast(int, x), CTRL, 0xf, 0xf, true));
}
__device__ __forceinline__ float row16_sum(float x) {
  x += dpp<0x128>(x); x += dpp<0x124>(x); x += dpp<0x122>(x); x += dpp<0x121>(x);
  return x;
}
__device__ __forceinline__ float row16_max(float x) {
  x = fmaxf(x, dpp<0x128>(x)); x = fmaxf(x, dpp<0x124>(x)); x = fmaxf(x, dpp<0x122>(x)); x = fmaxf(x, dpp<0x121>(x));
  return x;
}
__device__ __forceinline__ float xrow_sum(float x) {
  auto s = __builtin_amdgcn_permlane16_swap(__float_as_uint(x), __float_as_uint(x), false, false);
  x = __uint_as_float(s[0]) + __uint_as_float(s[1]);
  auto t = __builtin_amdgcn_permlane32_swap(__float_as_uint(x), __float_as_uint(x), false, false);
  return __uint_as_float(t[0]) + __uint_as_float(t[1]);
}
__device__ __forceinline__ float swap16_sum(float a, float b) {
  auto s = __builtin_amdgcn_permlane16_swap(__float_as_uint(a), __float_as_uint(b), false, false);
  return __uint_as_float(s[0]) + __uint_as_float(s[1]);
}
__device__ __forceinline__ float swap32_sum(float a, float b) {
  auto s = __builtin_amdgcn_permlane32_swap(__float_as_uint(a), __float_as_uint(b), false, false);
  return __uint_as_float(s[0]) + __uint_as_float(s[1]);
}
__device__ __forceinline__ float xrow_max(float x) {
  auto s = __builtin_amdgcn_permlane16_swap(__float_as_uint(x), __float_as_uint(x), false, false);
  x = fmaxf(__uint_as_float(s[0]), __uint_as_float(s[1]));
  auto t = __builtin_amdgcn_permlane32_swap(__float_as_uint(x), __float_as_uint(x), false, false);
  return fmaxf(__uint_as_float(t[0]), __uint_as_float(t[1]));
}
__device__ __forceinline__ int opaque(int x) { asm volatile("" : "+s"(x)); return x; }
__device__ __forceinline__ float bf_lo(unsigned u) { return __uint_as_float(u << 16); }
__device__ __forceinline__ float bf_hi(unsigned u) { return __uint_as_float(u & 0xffff0000u); }

__global__ void __launch_bounds__(256, 2)
linf_kernel(const unsigned short* __restrict__ q, const unsigned short* __restrict__ k, const unsigned short* __restrict__ v,
     const unsigned short* __restrict__ e, const unsigned short* __restrict__ f, __hip_bfloat16* __restrict__ out) {
  constexpr int NW = 4, BPW = 2, XAUX = 2, PAUX = 0, ROT = 3, SM = 2, MT = 2;
  constexpr int ND = SKV / (64 * NW);
  constexpr int NS = 2 * ND;
  __shared__ __attribute__((aligned(16))) char smem[NW * 16384 + (BPW * NW * RK + BPW * NW * DH + NW * RK) * 4];
  float* lds = reinterpret_cast<float*>(smem + NW * 16384);
  const int wave = __builtin_amdgcn_readfirstlane(threadIdx.x >> 6);
  const int bh0 = ((blockIdx.x & 7) * (NBH / BPW / 8) + (blockIdx.x >> 3)) * BPW;
  const __amdgpu_buffer_rsrc_t rk = __builtin_amdgcn_make_buffer_rsrc((void*)(k + (size_t)bh0 * (SKV * DH)), 0, XBYTES * BPW, 0x00020000);
  const __amdgpu_buffer_rsrc_t rv = __builtin_amdgcn_make_buffer_rsrc((void*)(v + (size_t)bh0 * (SKV * DH)), 0, XBYTES * BPW, 0x00020000);
  const __amdgpu_buffer_rsrc_t re = __builtin_amdgcn_make_buffer_rsrc((void*)e, 0, PBYTES, 0x00020000);
  const __amdgpu_buffer_rsrc_t rf = __builtin_amdgcn_make_buffer_rsrc((void*)f, 0, PBYTES, 0x00020000);
  const int par = (wave ^ (blockIdx.x >> 3)) & 1;
  int xoff, poff, wr, rd;
  u32x4 qq[2];
  {
    const int lane = threadIdx.x & 63, g = lane >> 4, c = lane & 15;
    xoff = g * 2048 + c * 16;
    poff = (lane >> 3) * 8192 + (lane & 7) * 16;
    wr = wave * 16384 + (lane >> 3) * 256 + (((lane & 7) ^ (lane >> 3)) * 16);
    rd = wave * 16384 + c * 128 + ((g ^ (c >> 1) ^ (par * 4)) * 16);
  }
  const int rot = blockIdx.x * ROT;
  u32x4 X[8], R[4];
#define BLK(S) ((((((S) >> 1) + rot) & (ND - 1)) * NW + wave))
#define XS(S) (((S) / (2 * NS)) * XBYTES + BLK(S) * 16384 + ((((S) ^ par) & 1) * 8192))
#define LDX(rs, so, i) __builtin_amdgcn_raw_buffer_load_b128(rs, xoff + (i) * 256, so, XAUX)
#define PS(T) (((T) & 1) * (32 * 4096) + BLK(T) * 128 + (((T) >= 2 * BPW * NS) ? PBYTES : 0))
#define LDP(rs, so, i) __builtin_amdgcn_raw_buffer_load_b128(rs, poff, (so) + ((i) >> 1) * (16 * 4096) + ((i) & 1) * 4096, PAUX)
#define WRP(T, i, val) *reinterpret_cast<u32x4*>(smem + (wr + ((T) & 3) * 4096) + ((i) >> 1) * 2048 + ((i) & 1) * 128) = (val)
  {
    u32x4 R2[4];
#pragma unroll
    for (int i = 0; i < 8; ++i) X[i] = LDX(rk, XS(0), i);
    __builtin_amdgcn_sched_barrier(0);
#pragma unroll
    for (int i = 0; i < 4; ++i) R[i] = LDP(re, PS(0), i);
#pragma unroll
    for (int i = 0; i < 4; ++i) R2[i] = LDP(re, PS(1), i);
    qq[0] = *reinterpret_cast<const u32x4*>(q + (size_t)bh0 * DH + 8 * (threadIdx.x & 15));
    qq[1] = *reinterpret_cast<const u32x4*>(q + (size_t)(bh0 + 1) * DH + 8 * (threadIdx.x & 15));
    __builtin_amdgcn_sched_barrier(0);
#pragma unroll
    for (int i = 0; i < 4; ++i) { WRP(0, i, R[i]); R[i] = LDP(re, PS(2), i); }
#pragma unroll
    for (int i = 0; i < 4; ++i) WRP(1, i, R2[i]);
    asm volatile("" : "+v"(qq[0]), "+v"(qq[1]));
  }
  f32x4 acc[4][8];
  int S = 0;
#pragma unroll 1
  for (int seg = 0; seg < 3 * BPW; ++seg) {
    const int b = seg >= 3, ty = seg - 3 * b;
    const int send = b * 2 * NS + (ty == 0 ? NS : ty == 1 ? NS + SM : 2 * NS - b * MT);
    int tid = threadIdx.x;
    asm volatile("" : "+v"(tid));
    const int lane = tid & 63, g = lane >> 4, c = lane & 15;
    float* sc = lds + b * (NW * RK);
    float* pw = lds + 2 * NW * RK + wave * RK;
    float* op = lds + 3 * NW * RK;
    if (opaque(ty) == 2) {
      __builtin_amdgcn_fence(__ATOMIC_RELEASE, "workgroup", "local");
      __builtin_amdgcn_s_barrier();
      __builtin_amdgcn_fence(__ATOMIC_ACQUIRE, "workgroup", "local");
      float s = 0.f;
#pragma unroll
      for (int w = 0; w < NW; ++w) s += sc[w * RK + lane];
      s *= 0.08838834764831845f * 1.4426950408889634f;
      const float m = xrow_max(row16_max(s));
      const float ex = __builtin_amdgcn_exp2f(s - m);
      const float sum = xrow_sum(row16_sum(ex));
      pw[lane] = ex / sum;
    }
    {
      if (opaque(ty) == 1) {
        const u32x4 qv = b ? qq[1] : qq[0];
        float qf[8];
#pragma unroll
        for (int d = 0; d < 4; ++d) { qf[2 * d] = bf_lo(qv[d]); qf[2 * d + 1] = bf_hi(qv[d]); }
#pragma unroll
        for (int t = 0; t < 4; ++t) {
          f32x4 sv;
#pragma unroll
          for (int r = 0; r < 4; ++r) {
            float x = 0.f;
#pragma unroll
            for (int j = 0; j < 8; ++j) x = fmaf(qf[j], acc[t][j][r], x);
            sv[r] = row16_sum(x);
          }
          if (c == 0) *reinterpret_cast<f32x4*>(sc + wave * RK + 16 * t + 4 * g) = sv;
        }
      }
      if (opaque(seg) == 3) {
        __builtin_amdgcn_fence(__ATOMIC_RELEASE, "wavefront", "local");
        __builtin_amdgcn_wave_barrier();
        __builtin_amdgcn_fence(__ATOMIC_ACQUIRE, "wavefront", "local");
        float o[8];
#pragma unroll
        for (int j = 0; j < 8; ++j) o[j] = 0.f;
#pragma unroll
        for (int t = 0; t < 4; ++t) {
          const f32x4 pv = *reinterpret_cast<const f32x4*>(pw + 16 * t + 4 * g);
#pragma unroll
          for (int j = 0; j < 8; ++j)
#pragma unroll
            for (int r = 0; r < 4; ++r) o[j] = fmaf(pv[r], acc[t][j][r], o[j]);
        }
        const float y0 = swap16_sum(o[0], o[4]), y1 = swap16_sum(o[1], o[5]);
        const float y2 = swap16_sum(o[2], o[6]), y3 = swap16_sum(o[3], o[7]);
        *reinterpret_cast<f32x2*>(op + wave * DH + 8 * c + 4 * (g & 1) + (g & 2)) = f32x2{swap32_sum(y0, y2), swap32_sum(y1, y3)};
      }
      if (opaque(ty) != 2) {
#pragma unroll
        for (int t = 0; t < 4; ++t)
#pragma unroll
          for (int j = 0; j < 8; ++j) acc[t][j] = f32x4{0.f, 0.f, 0.f, 0.f};
      }
    }
#pragma unroll 1
    for (; S < send; ++S) {
      const __amdgpu_buffer_rsrc_t rx = (((S + 1) / NS) & 1) ? rv : rk;
      const __amdgpu_buffer_rsrc_t rp = (((S + 3) / NS) & 1) ? rf : re;
      const int xs = XS(S + 1), ps = PS(S + 3);
      asm volatile("" : "+v"(xoff));
      u32x4 B[8], P[4];
      const char* rp_lds = smem + (rd ^ (((S & 1) << 6) | ((S & 2) << 12)));
#pragma unroll
      for (int t = 0; t < 4; ++t) P[t] = *reinterpret_cast<const u32x4*>(rp_lds + t * 2048);
      __builtin_amdgcn_sched_barrier(0);
#pragma unroll
      for (int u = 0; u < 4; ++u) {
#pragma unroll
        for (int d = 0; d < 4; ++d) {
          B[2 * d][u]     = __builtin_amdgcn_perm(X[2 * u + 1][d], X[2 * u][d], 0x05040100u);
          B[2 * d + 1][u] = __builtin_amdgcn_perm(X[2 * u + 1][d], X[2 * u][d], 0x07060302u);
        }
        __builtin_amdgcn_sched_barrier(0);
        X[2 * u] = LDX(rx, xs, 2 * u); X[2 * u + 1] = LDX(rx, xs, 2 * u + 1);
        __builtin_amdgcn_sched_barrier(0);
      }
#pragma unroll
      for (int t = 0; t < 4; ++t) {
        WRP(S + 2, t, R[t]);
        R[t] = LDP(rp, ps, t);
#pragma unroll
        for (int j = 0; j < 8; ++j)
          acc[t][j] = __builtin_amdgcn_mfma_f32_16x16x32_bf16(__builtin_bit_cast(bf16x8, P[t]), __builtin_bit_cast(bf16x8, B[j]), acc[t][j], 0, 0, 0);
        __builtin_amdgcn_sched_barrier(0);
      }
    }
  }
  {
    constexpr int SE = 2 * BPW * NS;
    int tid = threadIdx.x;
    asm volatile("" : "+v"(tid));
    const int lane = tid & 63, g = lane >> 4, c = lane & 15;
    const float* pw = lds + 2 * NW * RK + wave * RK;
    float* op = lds + 3 * NW * RK + (BPW - 1) * (NW * DH);
    u32x4 Y[MT][8], R2[4];
#pragma unroll
    for (int i = 0; i < 8; ++i) Y[0][i] = X[i];
    if (MT == 4) {
#pragma unroll
      for (int i = 0; i < 4; ++i) R2[i] = LDP(rf, PS(SE - 1), i);
    }
    if (MT > 1) {
#pragma unroll
      for (int i = 0; i < 8; ++i) Y[1 % MT][i] = LDX(rv, XS(SE - MT + 1), i);
    }
    __builtin_amdgcn_sched_barrier(0);
    __builtin_amdgcn_fence(__ATOMIC_RELEASE, "wavefront", "local");
    __builtin_amdgcn_wave_barrier();
    __builtin_amdgcn_fence(__ATOMIC_ACQUIRE, "wavefront", "local");
    f32x2 o[4];
#pragma unroll
    for (int j = 0; j < 4; ++j) o[j] = f32x2{0.f, 0.f};
#pragma unroll
    for (int t = 0; t < 4; ++t) {
      const f32x4 pv = *reinterpret_cast<const f32x4*>(pw + 16 * t + 4 * g);
#pragma unroll
      for (int j = 0; j < 8; ++j)
#pragma unroll
        for (int r = 0; r < 4; ++r) o[j >> 1][j & 1] = fmaf(pv[r], acc[t][j][r], o[j >> 1][j & 1]);
      asm volatile("" : "+v"(o[0]), "+v"(o[1]), "+v"(o[2]), "+v"(o[3]), "+v"(xoff));
      __builtin_amdgcn_sched_barrier(0);
      if (t + 2 < MT) {
#pragma unroll
        for (int i = 0; i < 8; ++i) Y[(t + 2) % MT][i] = LDX(rv, XS(SE - MT + t + 2), i);
      }
      __builtin_amdgcn_sched_barrier(0);
    }
    float pc[4];
#pragma unroll
    for (int t = 0; t < 4; ++t) pc[t] = pw[16 * t + c];
    float w[MT][8];
    auto calc_w = [&](int s) {
      const int Sx = SE - MT + s;
      const char* rp_lds = smem + (rd ^ (((Sx & 1) << 6) | ((Sx & 2) << 12)));
      f32x2 x[4];
#pragma unroll
      for (int t = 0; t < 4; ++t) {
        const u32x4 P = *reinterpret_cast<const u32x4*>(rp_lds + t * 2048);
#pragma unroll
        for (int d = 0; d < 4; ++d) {
          const f32x2 fv = f32x2{bf_lo(P[d]), bf_hi(P[d])};
          x[d] = t == 0 ? fv * pc[0] : __builtin_elementwise_fma(fv, f32x2{pc[t], pc[t]}, x[d]);
        }
      }
#pragma unroll
      for (int i = 0; i < 8; ++i) w[s][i] = row16_sum(x[i >> 1][i & 1]);
    };
    auto upd = [&](int s) {
#pragma unroll
      for (int i = 0; i < 8; ++i)
#pragma unroll
        for (int d = 0; d < 4; ++d)
          o[d] = __builtin_elementwise_fma(f32x2{bf_lo(Y[s][i][d]), bf_hi(Y[s][i][d])}, f32x2{w[s][i], w[s][i]}, o[d]);
    };
    constexpr int EARLY = MT > 2 ? MT - 2 : MT;
#pragma unroll
    for (int s = 0; s < EARLY; ++s) calc_w(s);
    __builtin_amdgcn_sched_barrier(0);
    if (MT > 2) {
#pragma unroll
      for (int i = 0; i < 4; ++i) WRP(SE - MT + 2, i, R[i]);
      if (MT == 4) {
#pragma unroll
        for (int i = 0; i < 4; ++i) WRP(SE - 1, i, R2[i]);
      }
      __builtin_amdgcn_sched_barrier(0);
    }
    upd(0);
    __builtin_amdgcn_sched_barrier(0);
#pragma unroll
    for (int s = EARLY; s < MT; ++s) calc_w(s);
    __builtin_amdgcn_sched_barrier(0);
#pragma unroll
    for (int s = 1; s < MT; ++s) { upd(s); __builtin_amdgcn_sched_barrier(0); }
    const float y0 = swap16_sum(o[0][0], o[2][0]), y1 = swap16_sum(o[0][1], o[2][1]);
    const float y2 = swap16_sum(o[1][0], o[3][0]), y3 = swap16_sum(o[1][1], o[3][1]);
    *reinterpret_cast<f32x2*>(op + wave * DH + 8 * c + 4 * (g & 1) + (g & 2)) = f32x2{swap32_sum(y0, y2), swap32_sum(y1, y3)};
  }
  __builtin_amdgcn_fence(__ATOMIC_RELEASE, "workgroup", "local");
  __builtin_amdgcn_s_barrier();
  __builtin_amdgcn_fence(__ATOMIC_ACQUIRE, "workgroup", "local");
  {
    const int tid = threadIdx.x;
    const float* op = lds + 3 * NW * RK + (tid >> 7) * (NW * DH) + (tid & 127);
    float r = 0.f;
#pragma unroll
    for (int w = 0; w < NW; ++w) r += op[w * DH];
    out[(size_t)bh0 * DH + tid] = __float2bfloat16(r);
  }
}
}

extern "C" void kernel_launch(void* const* d_in, const int* in_sizes, int n_in, void* d_out, int out_size, void* d_ws, size_t ws_size,
                              hipStream_t stream) {
  (void)in_sizes; (void)n_in; (void)out_size; (void)d_ws; (void)ws_size;
  lf::linf_kernel<<<lf::NBH / 2, 256, 0, stream>>>(
      (const unsigned short*)d_in[0], (const unsigned short*)d_in[1], (const unsigned short*)d_in[2],
      (const unsigned short*)d_in[3], (const unsigned short*)d_in[4], (__hip_bfloat16*)d_out);
}
```

```cpp
#include <hip/hip_runtime.h>
#include <hip/hip_bf16.h>
#include <stdint.h>

namespace lf {
typedef short bf16x8 __attribute__((ext_vector_type(8)));
typedef float f32x4 __attribute__((ext_vector_type(4)));
typedef float f32x2 __attribute__((ext_vector_type(2)));
typedef unsigned u32x4 __attribute__((ext_vector_type(4)));

constexpr int NBH = 512, SKV = 2048, DH = 128, RK = 64;
constexpr unsigned XBYTES = SKV * DH * 2;
constexpr unsigned PBYTES = RK * SKV * 2;

template <int CTRL> __device__ __forceinline__ float dpp(float x) {
  return __builtin_bit_cast(float, __builtin_amdgcn_mov_dpp(__builtin_bit_cast(int, x), CTRL, 0xf, 0xf, true));
}
__device__ __forceinline__ float row16_sum(float x) {
  x += dpp<0x128>(x); x += dpp<0x124>(x); x += dpp<0x122>(x); x += dpp<0x121>(x);
  return x;
}
__device__ __forceinline__ float row16_max(float x) {
  x = fmaxf(x, dpp<0x128>(x)); x = fmaxf(x, dpp<0x124>(x)); x = fmaxf(x, dpp<0x122>(x)); x = fmaxf(x, dpp<0x121>(x));
  return x;
}
__device__ __forceinline__ float xrow_sum(float x) {
  auto s = __builtin_amdgcn_permlane16_swap(__float_as_uint(x), __float_as_uint(x), false, false);
  x = __uint_as_float(s[0]) + __uint_as_float(s[1]);
  auto t = __builtin_amdgcn_permlane32_swap(__float_as_uint(x), __float_as_uint(x), false, false);
  return __uint_as_float(t[0]) + __uint_as_float(t[1]);
}
__device__ __forceinline__ float swap16_sum(float a, float b) {
  auto s = __builtin_amdgcn_permlane16_swap(__float_as_uint(a), __float_as_uint(b), false, false);
  return __uint_as_float(s[0]) + __uint_as_float(s[1]);
}
__device__ __forceinline__ float swap32_sum(float a, float b) {
  auto s = __builtin_amdgcn_permlane32_swap(__float_as_uint(a), __float_as_uint(b), false, false);
  return __uint_as_float(s[0]) + __uint_as_float(s[1]);
}
__device__ __forceinline__ float xrow_max(float x) {
  auto s = __builtin_amdgcn_permlane16_swap(__float_as_uint(x), __float_as_uint(x), false, false);
  x = fmaxf(__uint_as_float(s[0]), __uint_as_float(s[1]));
  auto t = __builtin_amdgcn_permlane32_swap(__float_as_uint(x), __float_as_uint(x), false, false);
  return fmaxf(__uint_as_float(t[0]), __uint_as_float(t[1]));
}
__device__ __forceinline__ int opaque(int x) { asm volatile("" : "+s"(x)); return x; }
__device__ __forceinline__ float bf_lo(unsigned u) { return __uint_as_float(u << 16); }
__device__ __forceinline__ float bf_hi(unsigned u) { return __uint_as_float(u & 0xffff0000u); }

__global__ void __launch_bounds__(256, 2)
linf_kernel(const unsigned short* __restrict__ q, const unsigned short* __restrict__ k, const unsigned short* __restrict__ v,
     const unsigned short* __restrict__ e, const unsigned short* __restrict__ f, __hip_bfloat16* __restrict__ out) {
  constexpr int NW = 4, BPW = 2, XAUX = 2, PAUX = 0, ROT = 3, SM = 2, MT = 1;
  constexpr int ND = SKV / (64 * NW);
  constexpr int NS = 2 * ND;
  __shared__ __attribute__((aligned(16))) char smem[NW * 16384 + (BPW * NW * RK + BPW * NW * DH + NW * RK) * 4];
  float* lds = reinterpret_cast<float*>(smem + NW * 16384);
  const int wave = __builtin_amdgcn_readfirstlane(threadIdx.x >> 6);
  const int bh0 = ((blockIdx.x & 7) * (NBH / BPW / 8) + (blockIdx.x >> 3)) * BPW;
  const __amdgpu_buffer_rsrc_t rk = __builtin_amdgcn_make_buffer_rsrc((void*)(k + (size_t)bh0 * (SKV * DH)), 0, XBYTES * BPW, 0x00020000);
  const __amdgpu_buffer_rsrc_t rv = __builtin_amdgcn_make_buffer_rsrc((void*)(v + (size_t)bh0 * (SKV * DH)), 0, XBYTES * BPW, 0x00020000);
  const __amdgpu_buffer_rsrc_t re = __builtin_amdgcn_make_buffer_rsrc((void*)e, 0, PBYTES, 0x00020000);
  const __amdgpu_buffer_rsrc_t rf = __builtin_amdgcn_make_buffer_rsrc((void*)f, 0, PBYTES, 0x00020000);
  const int par = (wave ^ (blockIdx.x >> 3)) & 1;
  int xoff, poff, wr, rd;
  u32x4 qq[2];
  {
    const int lane = threadIdx.x & 63, g = lane >> 4, c = lane & 15;
    xoff = g * 2048 + c * 16;
    poff = (lane >> 3) * 8192 + (lane & 7) * 16;
    wr = wave * 16384 + (lane >> 3) * 256 + (((lane & 7) ^ (lane >> 3)) * 16);
    rd = wave * 16384 + c * 128 + ((g ^ (c >> 1) ^ (par * 4)) * 16);
  }
  const int rot = blockIdx.x * ROT;
  u32x4 X[8], R[4];
#define BLK(S) ((((((S) >> 1) + rot) & (ND - 1)) * NW + wave))
#define XS(S) (((S) / (2 * NS)) * XBYTES + BLK(S) * 16384 + ((((S) ^ par) & 1) * 8192))
#define LDX(rs, so, i) __builtin_amdgcn_raw_buffer_load_b128(rs, xoff + (i) * 256, so, XAUX)
#define PS(T) (((T) & 1) * (32 * 4096) + BLK(T) * 128 + (((T) >= 2 * BPW * NS) ? PBYTES : 0))
#define LDP(rs, so, i) __builtin_amdgcn_raw_buffer_load_b128(rs, poff, (so) + ((i) >> 1) * (16 * 4096) + ((i) & 1) * 4096, PAUX)
#define WRP(T, i, val) *reinterpret_cast<u32x4*>(smem + (wr + ((T) & 3) * 4096) + ((i) >> 1) * 2048 + ((i) & 1) * 128) = (val)
  f32x4 acc[4][8];
  {
    u32x4 Y[8], R0[4], R1[4], B[8], P[4];
#pragma unroll
    for (int i = 0; i < 8; ++i) Y[i] = LDX(rk, XS(0), i);
    __builtin_amdgcn_sched_barrier(0);
#pragma unroll
    for (int i = 0; i < 4; ++i) R0[i] = LDP(re, PS(0), i);
#pragma unroll
    for (int i = 0; i < 4; ++i) R1[i] = LDP(re, PS(1), i);
#pragma unroll
    for (int i = 0; i < 4; ++i) R[i] = LDP(re, PS(2), i);
    qq[0] = *reinterpret_cast<const u32x4*>(q + (size_t)bh0 * DH + 8 * (threadIdx.x & 15));
    qq[1] = *reinterpret_cast<const u32x4*>(q + (size_t)(bh0 + 1) * DH + 8 * (threadIdx.x & 15));
    __builtin_amdgcn_sched_barrier(0);
#pragma unroll
    for (int i = 0; i < 8; ++i) X[i] = LDX(rk, XS(1), i);
    __builtin_amdgcn_sched_barrier(0);
#pragma unroll
    for (int i = 0; i < 4; ++i) WRP(0, i, R0[i]);
#pragma unroll
    for (int i = 0; i < 4; ++i) WRP(1, i, R1[i]);
#pragma unroll
    for (int t = 0; t < 4; ++t) P[t] = *reinterpret_cast<const u32x4*>(smem + rd + t * 2048);
    __builtin_amdgcn_sched_barrier(0);
#pragma unroll
    for (int u = 0; u < 4; ++u)
#pragma unroll
      for (int d = 0; d < 4; ++d) {
        B[2 * d][u]     = __builtin_amdgcn_perm(Y[2 * u + 1][d], Y[2 * u][d], 0x05040100u);
        B[2 * d + 1][u] = __builtin_amdgcn_perm(Y[2 * u + 1][d], Y[2 * u][d], 0x07060302u);
      }
    asm volatile("" : "+v"(qq[0]), "+v"(qq[1]));
    __builtin_amdgcn_sched_barrier(0);
#pragma unroll
    for (int t = 0; t < 4; ++t) {
      WRP(2, t, R[t]);
      R[t] = LDP(re, PS(3), t);
#pragma unroll
      for (int j = 0; j < 8; ++j)
        acc[t][j] = __builtin_amdgcn_mfma_f32_16x16x32_bf16(__builtin_bit_cast(bf16x8, P[t]), __builtin_bit_cast(bf16x8, B[j]), f32x4{0.f, 0.f, 0.f, 0.f}, 0, 0, 0);
      __builtin_amdgcn_sched_barrier(0);
    }
  }
  int S = 1;
#pragma unroll 1
  for (int seg = 0; seg < 3 * BPW; ++seg) {
    const int b = seg >= 3, ty = seg - 3 * b;
    const int send = (int)(0x3f3230201210ull >> (8 * seg)) & 0xff;
#pragma unroll 1
    for (; S < send; ++S) {
      const __amdgpu_buffer_rsrc_t rx = (((S + 1) / NS) & 1) ? rv : rk;
      const __amdgpu_buffer_rsrc_t rp = (((S + 3) / NS) & 1) ? rf : re;
      const int xs = XS(S + 1), ps = PS(S + 3);
      asm volatile("" : "+v"(xoff));
      u32x4 B[8], P[4];
      const char* rp_lds = smem + (rd ^ (((S & 1) << 6) | ((S & 2) << 12)));
#pragma unroll
      for (int t = 0; t < 4; ++t) P[t] = *reinterpret_cast<const u32x4*>(rp_lds + t * 2048);
      __builtin_amdgcn_sched_barrier(0);
#pragma unroll
      for (int u = 0; u < 4; ++u) {
#pragma unroll
        for (int d = 0; d < 4; ++d) {
          B[2 * d][u]     = __builtin_amdgcn_perm(X[2 * u + 1][d], X[2 * u][d], 0x05040100u);
          B[2 * d + 1][u] = __builtin_amdgcn_perm(X[2 * u + 1][d], X[2 * u][d], 0x07060302u);
        }
        __builtin_amdgcn_sched_barrier(0);
        X[2 * u] = LDX(rx, xs, 2 * u); X[2 * u + 1] = LDX(rx, xs, 2 * u + 1);
        __builtin_amdgcn_sched_barrier(0);
      }
#pragma unroll
      for (int t = 0; t < 4; ++t) {
        WRP(S + 2, t, R[t]);
        R[t] = LDP(rp, ps, t);
#pragma unroll
        for (int j = 0; j < 8; ++j)
          acc[t][j] = __builtin_amdgcn_mfma_f32_16x16x32_bf16(__builtin_bit_cast(bf16x8, P[t]), __builtin_bit_cast(bf16x8, B[j]), acc[t][j], 0, 0, 0);
        __builtin_amdgcn_sched_barrier(0);
      }
    }
    int tid = threadIdx.x;
    asm volatile("" : "+v"(tid));
    const int lane = tid & 63, g = lane >> 4, c = lane & 15;
    float* sc = lds + b * (NW * RK);
    float* pw = lds + 2 * NW * RK + wave * RK;
    float* op = lds + 3 * NW * RK;
    if (opaque(ty) == 1) {
      __builtin_amdgcn_fence(__ATOMIC_RELEASE, "workgroup", "local");
      __builtin_amdgcn_s_barrier();
      __builtin_amdgcn_fence(__ATOMIC_ACQUIRE, "workgroup", "local");
      float s = 0.f;
#pragma unroll
      for (int w = 0; w < NW; ++w) s += sc[w * RK + lane];
      s *= 0.08838834764831845f * 1.4426950408889634f;
      const float m = xrow_max(row16_max(s));
      const float ex = __builtin_amdgcn_exp2f(s - m);
      const float sum = xrow_sum(row16_sum(ex));
      pw[lane] = ex / sum;
    }
    {
      if (opaque(ty) == 0) {
        const u32x4 qv = b ? qq[1] : qq[0];
        float qf[8];
#pragma unroll
        for (int d = 0; d < 4; ++d) { qf[2 * d] = bf_lo(qv[d]); qf[2 * d + 1] = bf_hi(qv[d]); }
#pragma unroll
        for (int t = 0; t < 4; ++t) {
          f32x4 sv;
#pragma unroll
          for (int r = 0; r < 4; ++r) {
            float x = 0.f;
#pragma unroll
            for (int j = 0; j < 8; ++j) x = fmaf(qf[j], acc[t][j][r], x);
            sv[r] = row16_sum(x);
          }
          if (c == 0) *reinterpret_cast<f32x4*>(sc + wave * RK + 16 * t + 4 * g) = sv;
        }
      }
      if (opaque(seg) == 2) {
        __builtin_amdgcn_fence(__ATOMIC_RELEASE, "wavefront", "local");
        __builtin_amdgcn_wave_barrier();
        __builtin_amdgcn_fence(__ATOMIC_ACQUIRE, "wavefront", "local");
        float o[8];
#pragma unroll
        for (int j = 0; j < 8; ++j) o[j] = 0.f;
#pragma unroll
        for (int t = 0; t < 4; ++t) {
          const f32x4 pv = *reinterpret_cast<const f32x4*>(pw + 16 * t + 4 * g);
#pragma unroll
          for (int j = 0; j < 8; ++j)
#pragma unroll
            for (int r = 0; r < 4; ++r) o[j] = fmaf(pv[r], acc[t][j][r], o[j]);
        }
        const float y0 = swap16_sum(o[0], o[4]), y1 = swap16_sum(o[1], o[5]);
        const float y2 = swap16_sum(o[2], o[6]), y3 = swap16_sum(o[3], o[7]);
        *reinterpret_cast<f32x2*>(op + wave * DH + 8 * c + 4 * (g & 1) + (g & 2)) = f32x2{swap32_sum(y0, y2), swap32_sum(y1, y3)};
      }
      if (opaque(seg) == 0 || seg == 2 || seg == 3) {
#pragma unroll
        for (int t = 0; t < 4; ++t)
#pragma unroll
          for (int j = 0; j < 8; ++j) acc[t][j] = f32x4{0.f, 0.f, 0.f, 0.f};
      }
    }
  }
  {
    constexpr int SL = 2 * BPW * NS - 1;
    int tid = threadIdx.x;
    asm volatile("" : "+v"(tid));
    const int lane = tid & 63, g = lane >> 4, c = lane & 15;
    const float* pw = lds + 2 * NW * RK + wave * RK;
    float* op = lds + 3 * NW * RK + (BPW - 1) * (NW * DH);
    const char* rp_lds = smem + (rd ^ (((SL & 1) << 6) | ((SL & 2) << 12)));
    f32x4 pv[4];
    float pc[4];
    u32x4 P[4];
#pragma unroll
    for (int t = 0; t < 4; ++t) pv[t] = *reinterpret_cast<const f32x4*>(pw + 16 * t + 4 * g);
#pragma unroll
    for (int t = 0; t < 4; ++t) pc[t] = pw[16 * t + c];
#pragma unroll
    for (int t = 0; t < 4; ++t) P[t] = *reinterpret_cast<const u32x4*>(rp_lds + t * 2048);
    __builtin_amdgcn_sched_barrier(0);
    f32x2 o2[8];
#pragma unroll
    for (int t = 0; t < 4; ++t)
#pragma unroll
      for (int h = 0; h < 2; ++h)
#pragma unroll
        for (int j = 0; j < 8; ++j) {
          const f32x2 pp = f32x2{pv[t][2 * h], pv[t][2 * h + 1]}, aa = f32x2{acc[t][j][2 * h], acc[t][j][2 * h + 1]};
          o2[j] = (t == 0 && h == 0) ? pp * aa : __builtin_elementwise_fma(pp, aa, o2[j]);
        }
    f32x2 o[4];
#pragma unroll
    for (int d = 0; d < 4; ++d) o[d] = f32x2{o2[2 * d][0] + o2[2 * d][1], o2[2 * d + 1][0] + o2[2 * d + 1][1]};
    asm volatile("" : "+v"(o[0]), "+v"(o[1]), "+v"(o[2]), "+v"(o[3]));
    __builtin_amdgcn_sched_barrier(0);
    f32x2 x[4];
#pragma unroll
    for (int t = 0; t < 4; ++t)
#pragma unroll
      for (int d = 0; d < 4; ++d) {
        const f32x2 fv = f32x2{bf_lo(P[t][d]), bf_hi(P[t][d])};
        x[d] = t == 0 ? fv * pc[0] : __builtin_elementwise_fma(fv, f32x2{pc[t], pc[t]}, x[d]);
      }
    float w[8];
#pragma unroll
    for (int i = 0; i < 8; ++i) w[i] = x[i >> 1][i & 1];
    __builtin_amdgcn_sched_barrier(0);
#pragma unroll
    for (int i = 0; i < 8; ++i) w[i] += dpp<0x128>(w[i]);
    __builtin_amdgcn_sched_barrier(0);
#pragma unroll
    for (int i = 0; i < 8; ++i) w[i] += dpp<0x124>(w[i]);
    __builtin_amdgcn_sched_barrier(0);
#pragma unroll
    for (int i = 0; i < 8; ++i) w[i] += dpp<0x122>(w[i]);
    __builtin_amdgcn_sched_barrier(0);
#pragma unroll
    for (int i = 0; i < 8; ++i) w[i] += dpp<0x121>(w[i]);
    asm volatile("" : "+v"(w[0]), "+v"(w[1]), "+v"(w[2]), "+v"(w[3]), "+v"(w[4]), "+v"(w[5]), "+v"(w[6]), "+v"(w[7]));
    __builtin_amdgcn_sched_barrier(0);
#pragma unroll
    for (int i = 0; i < 8; ++i) {
#pragma unroll
      for (int d = 0; d < 4; ++d)
        o[d] = __builtin_elementwise_fma(f32x2{bf_lo(X[i][d]), bf_hi(X[i][d])}, f32x2{w[i], w[i]}, o[d]);
      __builtin_amdgcn_sched_barrier(0);
    }
    asm volatile("" :: "v"(R[0]), "v"(R[1]), "v"(R[2]), "v"(R[3]));
    const float y0 = swap16_sum(o[0][0], o[2][0]), y1 = swap16_sum(o[0][1], o[2][1]);
    const float y2 = swap16_sum(o[1][0], o[3][0]), y3 = swap16_sum(o[1][1], o[3][1]);
    *reinterpret_cast<f32x2*>(op + wave * DH + 8 * c + 4 * (g & 1) + (g & 2)) = f32x2{swap32_sum(y0, y2), swap32_sum(y1, y3)};
  }
  __builtin_amdgcn_fence(__ATOMIC_RELEASE, "workgroup", "local");
  __builtin_amdgcn_s_barrier();
  __builtin_amdgcn_fence(__ATOMIC_ACQUIRE, "workgroup", "local");
  {
    const int tid = threadIdx.x;
    const float* op = lds + 3 * NW * RK + (tid >> 7) * (NW * DH) + (tid & 127);
    float r = 0.f;
#pragma unroll
    for (int w = 0; w < NW; ++w) r += op[w * DH];
    out[(size_t)bh0 * DH + tid] = __float2bfloat16(r);
  }
}
}

extern "C" void kernel_launch(void* const* d_in, const int* in_sizes, int n_in, void* d_out, int out_size, void* d_ws, size_t ws_size,
                              hipStream_t stream) {
  (void)in_sizes; (void)n_in; (void)out_size; (void)d_ws; (void)ws_size;
  lf::linf_kernel<<<lf::NBH / 2, 256, 0, stream>>>(
      (const unsigned short*)d_in[0], (const unsigned short*)d_in[1], (const unsigned short*)d_in[2],
      (const unsigned short*)d_in[3], (const unsigned short*)d_in[4], (__hip_bfloat16*)d_out);
}
```

```cpp
#include <hip/hip_runtime.h>
#include <hip/hip_bf16.h>
#include <stdint.h>

namespace lf {
typedef short bf16x8 __attribute__((ext_vector_type(8)));
typedef float f32x4 __attribute__((ext_vector_type(4)));
typedef float f32x2 __attribute__((ext_vector_type(2)));
typedef unsigned u32x4 __attribute__((ext_vector_type(4)));

constexpr int NBH = 512, SKV = 2048, DH = 128, RK = 64;
constexpr unsigned XBYTES = SKV * DH * 2;
constexpr unsigned PBYTES = RK * SKV * 2;

template <int CTRL> __device__ __forceinline__ float dpp(float x) {
  return __builtin_bit_cast(float, __builtin_amdgcn_mov_dpp(__builtin_bit_cast(int, x), CTRL, 0xf, 0xf, true));
}
__device__ __forceinline__ float row16_sum(float x) {
  x += dpp<0x128>(x); x += dpp<0x124>(x); x += dpp<0x122>(x); x += dpp<0x121>(x);
  return x;
}
__device__ __forceinline__ float row16_max(float x) {
  x = fmaxf(x, dpp<0x128>(x)); x = fmaxf(x, dpp<0x124>(x)); x = fmaxf(x, dpp<0x122>(x)); x = fmaxf(x, dpp<0x121>(x));
  return x;
}
__device__ __forceinline__ float xrow_sum(float x) {
  auto s = __builtin_amdgcn_permlane16_swap(__float_as_uint(x), __float_as_uint(x), false, false);
  x = __uint_as_float(s[0]) + __uint_as_float(s[1]);
  auto t = __builtin_amdgcn_permlane32_swap(__float_as_uint(x), __float_as_uint(x), false, false);
  return __uint_as_float(t[0]) + __uint_as_float(t[1]);
}
__device__ __forceinline__ float swap16_sum(float a, float b) {
  auto s = __builtin_amdgcn_permlane16_swap(__float_as_uint(a), __float_as_uint(b), false, false);
  return __uint_as_float(s[0]) + __uint_as_float(s[1]);
}
__device__ __forceinline__ float swap32_sum(float a, float b) {
  auto s = __builtin_amdgcn_permlane32_swap(__float_as_uint(a), __float_as_uint(b), false, false);
  return __uint_as_float(s[0]) + __uint_as_float(s[1]);
}
__device__ __forceinline__ float xrow_max(float x) {
  auto s = __builtin_amdgcn_permlane16_swap(__float_as_uint(x), __float_as_uint(x), false, false);
  x = fmaxf(__uint_as_float(s[0]), __uint_as_float(s[1]));
  auto t = __builtin_amdgcn_permlane32_swap(__float_as_uint(x), __float_as_uint(x), false, false);
  return fmaxf(__uint_as_float(t[0]), __uint_as_float(t[1]));
}
__device__ __forceinline__ int opaque(int x) { asm volatile("" : "+s"(x)); return x; }
__device__ __forceinline__ float bf_lo(unsigned u) { return __uint_as_float(u << 16); }
__device__ __forceinline__ float bf_hi(unsigned u) { return __uint_as_float(u & 0xffff0000u); }

__global__ void __launch_bounds__(256, 2)
linf_kernel(const unsigned short* __restrict__ q, const unsigned short* __restrict__ k, const unsigned short* __restrict__ v,
     const unsigned short* __restrict__ e, const unsigned short* __restrict__ f, __hip_bfloat16* __restrict__ out) {
  constexpr int NW = 4, BPW = 2, XAUX = 2, PAUX = 0, ROT = 3, SM = 2, MT = 1;
  constexpr int ND = SKV / (64 * NW);
  constexpr int NS = 2 * ND;
  __shared__ __attribute__((aligned(16))) char smem[NW * 16384 + (BPW * NW * RK + BPW * NW * DH + NW * RK) * 4];
  float* lds = reinterpret_cast<float*>(smem + NW * 16384);
  const int wave = __builtin_amdgcn_readfirstlane(threadIdx.x >> 6);
  const int bh0 = ((blockIdx.x & 7) * (NBH / BPW / 8) + (blockIdx.x >> 3)) * BPW;
  const __amdgpu_buffer_rsrc_t rk = __builtin_amdgcn_make_buffer_rsrc((void*)(k + (size_t)bh0 * (SKV * DH)), 0, XBYTES * BPW, 0x00020000);
  const __amdgpu_buffer_rsrc_t rv = __builtin_amdgcn_make_buffer_rsrc((void*)(v + (size_t)bh0 * (SKV * DH)), 0, XBYTES * BPW, 0x00020000);
  const __amdgpu_buffer_rsrc_t re = __builtin_amdgcn_make_buffer_rsrc((void*)e, 0, PBYTES, 0x00020000);
  const __amdgpu_buffer_rsrc_t rf = __builtin_amdgcn_make_buffer_rsrc((void*)f, 0, PBYTES, 0x00020000);
  const int par = (wave ^ (blockIdx.x >> 3)) & 1;
  int xoff, poff, wr, rd;
  u32x4 qq[2];
  {
    const int lane = threadIdx.x & 63, g = lane >> 4, c = lane & 15;
    xoff = g * 2048 + c * 16;
    poff = (lane >> 3) * 8192 + (lane & 7) * 16;
    wr = wave * 16384 + (lane >> 3) * 256 + (((lane & 7) ^ (lane >> 3)) * 16);
    rd = wave * 16384 + c * 128 + ((g ^ (c >> 1) ^ (par * 4)) * 16);
  }
  const int rot = blockIdx.x * ROT;
  u32x4 X[8], R[4];
#define BLK(S) ((((((S) >> 1) + rot) & (ND - 1)) * NW + wave))
#define XS(S) (((S) / (2 * NS)) * XBYTES + BLK(S) * 16384 + ((((S) ^ par) & 1) * 8192))
#define LDX(rs, so, i) __builtin_amdgcn_raw_buffer_load_b128(rs, xoff + (i) * 256, so, XAUX)
#define PS(T) (((T) & 1) * (32 * 4096) + BLK(T) * 128 + (((T) >= 2 * BPW * NS) ? PBYTES : 0))
#define LDP(rs, so, i) __builtin_amdgcn_raw_buffer_load_b128(rs, poff, (so) + ((i) >> 1) * (16 * 4096) + ((i) & 1) * 4096, PAUX)
#define WRP(T, i, val) *reinterpret_cast<u32x4*>(smem + (wr + ((T) & 3) * 4096) + ((i) >> 1) * 2048 + ((i) & 1) * 128) = (val)
  f32x4 acc[4][8];
  {
    u32x4 Y[8], R0[4], R1[4], B[8], P[4];
#pragma unroll
    for (int i = 0; i < 8; ++i) Y[i] = LDX(rk, XS(0), i);
    __builtin_amdgcn_sched_barrier(0);
#pragma unroll
    for (int i = 0; i < 4; ++i) R0[i] = LDP(re, PS(0), i);
#pragma unroll
    for (int i = 0; i < 4; ++i) R1[i] = LDP(re, PS(1), i);
#pragma unroll
    for (int i = 0; i < 4; ++i) R[i] = LDP(re, PS(2), i);
    qq[0] = *reinterpret_cast<const u32x4*>(q + (size_t)bh0 * DH + 8 * (threadIdx.x & 15));
    qq[1] = *reinterpret_cast<const u32x4*>(q + (size_t)(bh0 + 1) * DH + 8 * (threadIdx.x & 15));
    __builtin_amdgcn_sched_barrier(0);
#pragma unroll
    for (int i = 0; i < 8; ++i) X[i] = LDX(rk, XS(1), i);
    __builtin_amdgcn_sched_barrier(0);
#pragma unroll
    for (int i = 0; i < 4; ++i) WRP(0, i, R0[i]);
#pragma unroll
    for (int i = 0; i < 4; ++i) WRP(1, i, R1[i]);
#pragma unroll
    for (int t = 0; t < 4; ++t) P[t] = *reinterpret_cast<const u32x4*>(smem + rd + t * 2048);
    __builtin_amdgcn_sched_barrier(0);
#pragma unroll
    for (int u = 0; u < 4; ++u)
#pragma unroll
      for (int d = 0; d < 4; ++d) {
        B[2 * d][u]     = __builtin_amdgcn_perm(Y[2 * u + 1][d], Y[2 * u][d], 0x05040100u);
        B[2 * d + 1][u] = __builtin_amdgcn_perm(Y[2 * u + 1][d], Y[2 * u][d], 0x07060302u);
      }
    asm volatile("" : "+v"(qq[0]), "+v"(qq[1]));
    __builtin_amdgcn_sched_barrier(0);
#pragma unroll
    for (int t = 0; t < 4; ++t) {
      WRP(2, t, R[t]);
      R[t] = LDP(re, PS(3), t);
#pragma unroll
      for (int j = 0; j < 8; ++j)
        acc[t][j] = __builtin_amdgcn_mfma_f32_16x16x32_bf16(__builtin_bit_cast(bf16x8, P[t]), __builtin_bit_cast(bf16x8, B[j]), f32x4{0.f, 0.f, 0.f, 0.f}, 0, 0, 0);
      __builtin_amdgcn_sched_barrier(0);
    }
  }
  int S = 1;
#pragma unroll 1
  for (int seg = 0; seg < 3 * BPW; ++seg) {
    const int b = seg >= 3, ty = seg - 3 * b;
    const int send = (int)(0x3e3230201210ull >> (8 * seg)) & 0xff;
#pragma unroll 1
    for (; S < send; ++S) {
      const __amdgpu_buffer_rsrc_t rx = (((S + 1) / NS) & 1) ? rv : rk;
      const __amdgpu_buffer_rsrc_t rp = (((S + 3) / NS) & 1) ? rf : re;
      const int xs = XS(S + 1), ps = PS(S + 3);
      asm volatile("" : "+v"(xoff));
      u32x4 B[8], P[4];
      const char* rp_lds = smem + (rd ^ (((S & 1) << 6) | ((S & 2) << 12)));
#pragma unroll
      for (int t = 0; t < 4; ++t) P[t] = *reinterpret_cast<const u32x4*>(rp_lds + t * 2048);
      __builtin_amdgcn_sched_barrier(0);
#pragma unroll
      for (int u = 0; u < 4; ++u) {
#pragma unroll
        for (int d = 0; d < 4; ++d) {
          B[2 * d][u]     = __builtin_amdgcn_perm(X[2 * u + 1][d], X[2 * u][d], 0x05040100u);
          B[2 * d + 1][u] = __builtin_amdgcn_perm(X[2 * u + 1][d], X[2 * u][d], 0x07060302u);
        }
        __builtin_amdgcn_sched_barrier(0);
        X[2 * u] = LDX(rx, xs, 2 * u); X[2 * u + 1] = LDX(rx, xs, 2 * u + 1);
        __builtin_amdgcn_sched_barrier(0);
      }
#pragma unroll
      for (int t = 0; t < 4; ++t) {
        WRP(S + 2, t, R[t]);
        R[t] = LDP(rp, ps, t);
#pragma unroll
        for (int j = 0; j < 8; ++j)
          acc[t][j] = __builtin_amdgcn_mfma_f32_16x16x32_bf16(__builtin_bit_cast(bf16x8, P[t]), __builtin_bit_cast(bf16x8, B[j]), acc[t][j], 0, 0, 0);
        __builtin_amdgcn_sched_barrier(0);
      }
    }
    int tid = threadIdx.x;
    asm volatile("" : "+v"(tid));
    const int lane = tid & 63, g = lane >> 4, c = lane & 15;
    float* sc = lds + b * (NW * RK);
    float* pw = lds + 2 * NW * RK + wave * RK;
    float* op = lds + 3 * NW * RK;
    if (opaque(ty) == 1) {
      __builtin_amdgcn_fence(__ATOMIC_RELEASE, "workgroup", "local");
      __builtin_amdgcn_s_barrier();
      __builtin_amdgcn_fence(__ATOMIC_ACQUIRE, "workgroup", "local");
      float s = 0.f;
#pragma unroll
      for (int w = 0; w < NW; ++w) s += sc[w * RK + lane];
      s *= 0.08838834764831845f * 1.4426950408889634f;
      const float m = xrow_max(row16_max(s));
      const float ex = __builtin_amdgcn_exp2f(s - m);
      const float sum = xrow_sum(row16_sum(ex));
      pw[lane] = ex / sum;
    }
    {
      if (opaque(ty) == 0) {
        const u32x4 qv = b ? qq[1] : qq[0];
        float qf[8];
#pragma unroll
        for (int d = 0; d < 4; ++d) { qf[2 * d] = bf_lo(qv[d]); qf[2 * d + 1] = bf_hi(qv[d]); }
#pragma unroll
        for (int t = 0; t < 4; ++t) {
          f32x4 sv;
#pragma unroll
          for (int r = 0; r < 4; ++r) {
            float x = 0.f;
#pragma unroll
            for (int j = 0; j < 8; ++j) x = fmaf(qf[j], acc[t][j][r], x);
            sv[r] = row16_sum(x);
          }
          if (c == 0) *reinterpret_cast<f32x4*>(sc + wave * RK + 16 * t + 4 * g) = sv;
        }
      }
      if (opaque(seg) == 2) {
        __builtin_amdgcn_fence(__ATOMIC_RELEASE, "wavefront", "local");
        __builtin_amdgcn_wave_barrier();
        __builtin_amdgcn_fence(__ATOMIC_ACQUIRE, "wavefront", "local");
        float o[8];
#pragma unroll
        for (int j = 0; j < 8; ++j) o[j] = 0.f;
#pragma unroll
        for (int t = 0; t < 4; ++t) {
          const f32x4 pv = *reinterpret_cast<const f32x4*>(pw + 16 * t + 4 * g);
#pragma unroll
          for (int j = 0; j < 8; ++j)
#pragma unroll
            for (int r = 0; r < 4; ++r) o[j] = fmaf(pv[r], acc[t][j][r], o[j]);
        }
        const float y0 = swap16_sum(o[0], o[4]), y1 = swap16_sum(o[1], o[5]);
        const float y2 = swap16_sum(o[2], o[6]), y3 = swap16_sum(o[3], o[7]);
        *reinterpret_cast<f32x2*>(op + wave * DH + 8 * c + 4 * (g & 1) + (g & 2)) = f32x2{swap32_sum(y0, y2), swap32_sum(y1, y3)};
      }
      if (opaque(seg) == 0 || seg == 2 || seg == 3) {
#pragma unroll
        for (int t = 0; t < 4; ++t)
#pragma unroll
          for (int j = 0; j < 8; ++j) acc[t][j] = f32x4{0.f, 0.f, 0.f, 0.f};
      }
    }
  }
  {
    constexpr int SL = 2 * BPW * NS - 2;
    int tid = threadIdx.x;
    asm volatile("" : "+v"(tid));
    const int lane = tid & 63, g = lane >> 4, c = lane & 15;
    const float* pw = lds + 2 * NW * RK + wave * RK;
    float* op = lds + 3 * NW * RK + (BPW - 1) * (NW * DH);
    f32x4 pv[4];
    float pc[4];
    u32x4 P[2][4], Y[8];
#pragma unroll
    for (int t = 0; t < 4; ++t) pv[t] = *reinterpret_cast<const f32x4*>(pw + 16 * t + 4 * g);
#pragma unroll
    for (int t = 0; t < 4; ++t) pc[t] = pw[16 * t + c];
#pragma unroll
    for (int t = 0; t < 4; ++t) P[0][t] = *reinterpret_cast<const u32x4*>(smem + (rd ^ ((SL & 2) << 12)) + t * 2048);
    __builtin_amdgcn_sched_barrier(0);
    f32x2 o2[8];
#pragma unroll
    for (int t = 0; t < 4; ++t)
#pragma unroll
      for (int h = 0; h < 2; ++h)
#pragma unroll
        for (int j = 0; j < 8; ++j) {
          const f32x2 pp = f32x2{pv[t][2 * h], pv[t][2 * h + 1]}, aa = f32x2{acc[t][j][2 * h], acc[t][j][2 * h + 1]};
          o2[j] = (t == 0 && h == 0) ? pp * aa : __builtin_elementwise_fma(pp, aa, o2[j]);
        }
    asm volatile("" : "+v"(o2[0]), "+v"(o2[1]), "+v"(o2[2]), "+v"(o2[3]), "+v"(o2[4]), "+v"(o2[5]), "+v"(o2[6]), "+v"(o2[7]), "+v"(xoff));
    __builtin_amdgcn_sched_barrier(0);
#pragma unroll
    for (int t = 0; t < 4; ++t) P[1][t] = *reinterpret_cast<const u32x4*>(smem + (rd ^ (64 | ((SL & 2) << 12))) + t * 2048);
#pragma unroll
    for (int i = 0; i < 8; ++i) Y[i] = LDX(rv, XS(SL + 1), i);
    __builtin_amdgcn_sched_barrier(0);
    f32x2 o[4];
#pragma unroll
    for (int d = 0; d < 4; ++d) { float lo = o2[2 * d][0] + o2[2 * d][1], hi = o2[2 * d + 1][0] + o2[2 * d + 1][1]; asm("" : "+v"(lo), "+v"(hi)); o[d] = f32x2{lo, hi}; }
    float w[16];
#pragma unroll
    for (int s = 0; s < 2; ++s) {
      f32x2 x[4];
#pragma unroll
      for (int t = 0; t < 4; ++t)
#pragma unroll
        for (int d = 0; d < 4; ++d) {
          const f32x2 fv = f32x2{bf_lo(P[s][t][d]), bf_hi(P[s][t][d])};
          x[d] = t == 0 ? fv * pc[0] : __builtin_elementwise_fma(fv, f32x2{pc[t], pc[t]}, x[d]);
        }
#pragma unroll
      for (int i = 0; i < 8; ++i) w[8 * s + i] = x[i >> 1][i & 1];
    }
    __builtin_amdgcn_sched_barrier(0);
#pragma unroll
    for (int i = 0; i < 16; ++i) w[i] += dpp<0x128>(w[i]);
    __builtin_amdgcn_sched_barrier(0);
#pragma unroll
    for (int i = 0; i < 16; ++i) w[i] += dpp<0x124>(w[i]);
    __builtin_amdgcn_sched_barrier(0);
#pragma unroll
    for (int i = 0; i < 16; ++i) w[i] += dpp<0x122>(w[i]);
    __builtin_amdgcn_sched_barrier(0);
#pragma unroll
    for (int i = 0; i < 16; ++i) w[i] += dpp<0x121>(w[i]);
    asm volatile("" : "+v"(w[0]), "+v"(w[1]), "+v"(w[2]), "+v"(w[3]), "+v"(w[4]), "+v"(w[5]), "+v"(w[6]), "+v"(w[7]));
    asm volatile("" : "+v"(w[8]), "+v"(w[9]), "+v"(w[10]), "+v"(w[11]), "+v"(w[12]), "+v"(w[13]), "+v"(w[14]), "+v"(w[15]));
    __builtin_amdgcn_sched_barrier(0);
#pragma unroll
    for (int i = 0; i < 8; ++i) {
#pragma unroll
      for (int d = 0; d < 4; ++d)
        o[d] = __builtin_elementwise_fma(f32x2{bf_lo(X[i][d]), bf_hi(X[i][d])}, f32x2{w[i], w[i]}, o[d]);
      __builtin_amdgcn_sched_barrier(0);
    }
    asm volatile("" :: "v"(R[0]), "v"(R[1]), "v"(R[2]), "v"(R[3]));
#pragma unroll
    for (int i = 0; i < 8; ++i) {
#pragma unroll
      for (int d = 0; d < 4; ++d)
        o[d] = __builtin_elementwise_fma(f32x2{bf_lo(Y[i][d]), bf_hi(Y[i][d])}, f32x2{w[8 + i], w[8 + i]}, o[d]);
      __builtin_amdgcn_sched_barrier(0);
    }
    const float y0 = swap16_sum(o[0][0], o[2][0]), y1 = swap16_sum(o[0][1], o[2][1]);
    const float y2 = swap16_sum(o[1][0], o[3][0]), y3 = swap16_sum(o[1][1], o[3][1]);
    *reinterpret_cast<f32x2*>(op + wave * DH + 8 * c + 4 * (g & 1) + (g & 2)) = f32x2{swap32_sum(y0, y2), swap32_sum(y1, y3)};
  }
  __builtin_amdgcn_fence(__ATOMIC_RELEASE, "workgroup", "local");
  __builtin_amdgcn_s_barrier();
  __builtin_amdgcn_fence(__ATOMIC_ACQUIRE, "workgroup", "local");
  {
    const int tid = threadIdx.x;
    const float* op = lds + 3 * NW * RK + (tid >> 7) * (NW * DH) + (tid & 127);
    float r = 0.f;
#pragma unroll
    for (int w = 0; w < NW; ++w) r += op[w * DH];
    out[(size_t)bh0 * DH + tid] = __float2bfloat16(r);
  }
}
}

extern "C" void kernel_launch(void* const* d_in, const int* in_sizes, int n_in, void* d_out, int out_size, void* d_ws, size_t ws_size,
                              hipStream_t stream) {
  (void)in_sizes; (void)n_in; (void)out_size; (void)d_ws; (void)ws_size;
  lf::linf_kernel<<<lf::NBH / 2, 256, 0, stream>>>(
      (const unsigned short*)d_in[0], (const unsigned short*)d_in[1], (const unsigned short*)d_in[2],
      (const unsigned short*)d_in[3], (const unsigned short*)d_in[4], (__hip_bfloat16*)d_out);
}
```

```cpp
#include <hip/hip_runtime.h>
#include <hip/hip_bf16.h>
#include <stdint.h>

namespace lf {
typedef short bf16x8 __attribute__((ext_vector_type(8)));
typedef float f32x4 __attribute__((ext_vector_type(4)));
typedef float f32x2 __attribute__((ext_vector_type(2)));
typedef unsigned u32x4 __attribute__((ext_vector_type(4)));

constexpr int NBH = 512, SKV = 2048, DH = 128, RK = 64;
constexpr unsigned XBYTES = SKV * DH * 2;
constexpr unsigned PBYTES = RK * SKV * 2;

template <int CTRL> __device__ __forceinline__ float dpp(float x) {
  return __builtin_bit_cast(float, __builtin_amdgcn_mov_dpp(__builtin_bit_cast(int, x), CTRL, 0xf, 0xf, true));
}
__device__ __forceinline__ float row16_sum(float x) {
  x += dpp<0x128>(x); x += dpp<0x124>(x); x += dpp<0x122>(x); x += dpp<0x121>(x);
  return x;
}
__device__ __forceinline__ float row16_max(float x) {
  x = fmaxf(x, dpp<0x128>(x)); x = fmaxf(x, dpp<0x124>(x)); x = fmaxf(x, dpp<0x122>(x)); x = fmaxf(x, dpp<0x121>(x));
  return x;
}
__device__ __forceinline__ float xrow_sum(float x) {
  auto s = __builtin_amdgcn_permlane16_swap(__float_as_uint(x), __float_as_uint(x), false, false);
  x = __uint_as_float(s[0]) + __uint_as_float(s[1]);
  auto t = __builtin_amdgcn_permlane32_swap(__float_as_uint(x), __float_as_uint(x), false, false);
  return __uint_as_float(t[0]) + __uint_as_float(t[1]);
}
__device__ __forceinline__ float swap16_sum(float a, float b) {
  auto s = __builtin_amdgcn_permlane16_swap(__float_as_uint(a), __float_as_uint(b), false, false);
  return __uint_as_float(s[0]) + __uint_as_float(s[1]);
}
__device__ __forceinline__ float swap32_sum(float a, float b) {
  auto s = __builtin_amdgcn_permlane32_swap(__float_as_uint(a), __float_as_uint(b), false, false);
  return __uint_as_float(s[0]) + __uint_as_float(s[1]);
}
__device__ __forceinline__ float xrow_max(float x) {
  auto s = __builtin_amdgcn_permlane16_swap(__float_as_uint(x), __float_as_uint(x), false, false);
  x = fmaxf(__uint_as_float(s[0]), __uint_as_float(s[1]));
  auto t = __builtin_amdgcn_permlane32_swap(__float_as_uint(x), __float_as_uint(x), false, false);
  return fmaxf(__uint_as_float(t[0]), __uint_as_float(t[1]));
}
__device__ __forceinline__ int opaque(int x) { asm volatile("" : "+s"(x)); return x; }
__device__ __forceinline__ float bf_lo(unsigned u) { return __uint_as_float(u << 16); }
__device__ __forceinline__ float bf_hi(unsigned u) { return __uint_as_float(u & 0xffff0000u); }

__global__ void __launch_bounds__(256, 2)
linf_kernel(const unsigned short* __restrict__ q, const unsigned short* __restrict__ k, const unsigned short* __restrict__ v,
     const unsigned short* __restrict__ e, const unsigned short* __restrict__ f, __hip_bfloat16* __restrict__ out) {
  constexpr int NW = 4, BPW = 2, XAUX = 2, PAUX = 0, ROT = 3;
  constexpr int ND = SKV / (64 * NW);
  constexpr int NS = 2 * ND;
  __shared__ __attribute__((aligned(16))) char smem[NW * 16384 + (BPW * NW * RK + BPW * NW * DH + NW * RK) * 4];
  float* lds = reinterpret_cast<float*>(smem + NW * 16384);
  const int wave = __builtin_amdgcn_readfirstlane(threadIdx.x >> 6);
  const int bh0 = ((blockIdx.x & 7) * (NBH / BPW / 8) + (blockIdx.x >> 3)) * BPW;
  const __amdgpu_buffer_rsrc_t rk = __builtin_amdgcn_make_buffer_rsrc((void*)(k + (size_t)bh0 * (SKV * DH)), 0, XBYTES * BPW, 0x00020000);
  const __amdgpu_buffer_rsrc_t rv = __builtin_amdgcn_make_buffer_rsrc((void*)(v + (size_t)bh0 * (SKV * DH)), 0, XBYTES * BPW, 0x00020000);
  const __amdgpu_buffer_rsrc_t re = __builtin_amdgcn_make_buffer_rsrc((void*)e, 0, PBYTES, 0x00020000);
  const __amdgpu_buffer_rsrc_t rf = __builtin_amdgcn_make_buffer_rsrc((void*)f, 0, PBYTES, 0x00020000);
  const int par = (wave ^ (blockIdx.x >> 3)) & 1;
  int xoff, poff, wr, rd;
  u32x4 qq[2];
  {
    const int lane = threadIdx.x & 63, g = lane >> 4, c = lane & 15;
    xoff = g * 2048 + c * 16;
    poff = (lane >> 3) * 8192 + (lane & 7) * 16;
    wr = wave * 16384 + (lane >> 3) * 256 + (((lane & 7) ^ (lane >> 3)) * 16);
    rd = wave * 16384 + c * 128 + ((g ^ (c >> 1) ^ (par * 4)) * 16);
  }
  const int rot = blockIdx.x * ROT;
  u32x4 X[8], R[4];
#define BLK(S) ((((((S) >> 1) + rot) & (ND - 1)) * NW + wave))
#define XS(S) (((S) / (2 * NS)) * XBYTES + BLK(S) * 16384 + ((((S) ^ par) & 1) * 8192))
#define LDX(rs, so, i) __builtin_amdgcn_raw_buffer_load_b128(rs, xoff + (i) * 256, so, XAUX)
#define PS(T) (((T) & 1) * (32 * 4096) + BLK(T) * 128 + (((T) >= 2 * BPW * NS) ? PBYTES : 0))
#define LDP(rs, so, i) __builtin_amdgcn_raw_buffer_load_b128(rs, poff, (so) + ((i) >> 1) * (16 * 4096) + ((i) & 1) * 4096, PAUX)
#define WRP(T, i, val) *reinterpret_cast<u32x4*>(smem + (wr + ((T) & 3) * 4096) + ((i) >> 1) * 2048 + ((i) & 1) * 128) = (val)
  f32x4 acc[4][8];
  {
    u32x4 Y[8], R0[4], R1[4], B[8], P[4];
#pragma unroll
    for (int i = 0; i < 8; ++i) Y[i] = LDX(rk, XS(0), i);
    __builtin_amdgcn_sched_barrier(0);
#pragma unroll
    for (int i = 0; i < 4; ++i) R0[i] = LDP(re, PS(0), i);
#pragma unroll
    for (int i = 0; i < 4; ++i) R1[i] = LDP(re, PS(1), i);
#pragma unroll
    for (int i = 0; i < 4; ++i) R[i] = LDP(re, PS(2), i);
    qq[0] = *reinterpret_cast<const u32x4*>(q + (size_t)bh0 * DH + 8 * (threadIdx.x & 15));
    qq[1] = *reinterpret_cast<const u32x4*>(q + (size_t)(bh0 + 1) * DH + 8 * (threadIdx.x & 15));
    __builtin_amdgcn_sched_barrier(0);
#pragma unroll
    for (int i = 0; i < 8; ++i) X[i] = LDX(rk, XS(1), i);
    __builtin_amdgcn_sched_barrier(0);
#pragma unroll
    for (int i = 0; i < 4; ++i) WRP(0, i, R0[i]);
#pragma unroll
    for (int i = 0; i < 4; ++i) WRP(1, i, R1[i]);
#pragma unroll
    for (int t = 0; t < 4; ++t) P[t] = *reinterpret_cast<const u32x4*>(smem + rd + t * 2048);
    __builtin_amdgcn_sched_barrier(0);
#pragma unroll
    for (int u = 0; u < 4; ++u)
#pragma unroll
      for (int d = 0; d < 4; ++d) {
        B[2 * d][u]     = __builtin_amdgcn_perm(Y[2 * u + 1][d], Y[2 * u][d], 0x05040100u);
        B[2 * d + 1][u] = __builtin_amdgcn_perm(Y[2 * u + 1][d], Y[2 * u][d], 0x07060302u);
      }
    asm volatile("" : "+v"(qq[0]), "+v"(qq[1]));
    __builtin_amdgcn_sched_barrier(0);
#pragma unroll
    for (int t = 0; t < 4; ++t) {
      WRP(2, t, R[t]);
      R[t] = LDP(re, PS(3), t);
#pragma unroll
      for (int j = 0; j < 8; ++j)
        acc[t][j] = __builtin_amdgcn_mfma_f32_16x16x32_bf16(__builtin_bit_cast(bf16x8, P[t]), __builtin_bit_cast(bf16x8, B[j]), f32x4{0.f, 0.f, 0.f, 0.f}, 0, 0, 0);
      __builtin_amdgcn_sched_barrier(0);
    }
  }
  int S = 1;
#pragma unroll 1
  for (int seg = 0; seg < 3 * BPW; ++seg) {
    const int b = seg >= 3, ty = seg - 3 * b;
    const int send = (int)(0x3e3230201210ull >> (8 * seg)) & 0xff;
#pragma unroll 1
    for (; S < send; ++S) {
      const __amdgpu_buffer_rsrc_t rx = (((S + 1) / NS) & 1) ? rv : rk;
      const __amdgpu_buffer_rsrc_t rp = (((S + 3) / NS) & 1) ? rf : re;
      const int xs = XS(S + 1), ps = PS(S + 3);
      asm volatile("" : "+v"(xoff));
      u32x4 B[8], P[4];
      const char* rp_lds = smem + (rd ^ (((S & 1) << 6) | ((S & 2) << 12)));
#pragma unroll
      for (int t = 0; t < 4; ++t) P[t] = *reinterpret_cast<const u32x4*>(rp_lds + t * 2048);
      __builtin_amdgcn_sched_barrier(0);
#pragma unroll
      for (int u = 0; u < 4; ++u) {
#pragma unroll
        for (int d = 0; d < 4; ++d) {
          B[2 * d][u]     = __builtin_amdgcn_perm(X[2 * u + 1][d], X[2 * u][d], 0x05040100u);
          B[2 * d + 1][u] = __builtin_amdgcn_perm(X[2 * u + 1][d], X[2 * u][d], 0x07060302u);
        }
        __builtin_amdgcn_sched_barrier(0);
        X[2 * u] = LDX(rx, xs, 2 * u); X[2 * u + 1] = LDX(rx, xs, 2 * u + 1);
        __builtin_amdgcn_sched_barrier(0);
      }
#pragma unroll
      for (int t = 0; t < 4; ++t) {
        WRP(S + 2, t, R[t]);
        R[t] = LDP(rp, ps, t);
#pragma unroll
        for (int j = 0; j < 8; ++j)
          acc[t][j] = __builtin_amdgcn_mfma_f32_16x16x32_bf16(__builtin_bit_cast(bf16x8, P[t]), __builtin_bit_cast(bf16x8, B[j]), acc[t][j], 0, 0, 0);
        __builtin_amdgcn_sched_barrier(0);
      }
    }
    int tid = threadIdx.x;
    asm volatile("" : "+v"(tid));
    const int lane = tid & 63, g = lane >> 4, c = lane & 15;
    float* sc = lds + b * (NW * RK);
    float* pw = lds + 2 * NW * RK + wave * RK;
    float* op = lds + 3 * NW * RK;
    if (opaque(ty) == 1) {
      __builtin_amdgcn_fence(__ATOMIC_RELEASE, "workgroup", "local");
      __builtin_amdgcn_s_barrier();
      __builtin_amdgcn_fence(__ATOMIC_ACQUIRE, "workgroup", "local");
      float s = 0.f;
#pragma unroll
      for (int w = 0; w < NW; ++w) s += sc[w * RK + lane];
      s *= 0.08838834764831845f * 1.4426950408889634f;
      const float m = xrow_max(row16_max(s));
      const float ex = __builtin_amdgcn_exp2f(s - m);
      const float sum = xrow_sum(row16_sum(ex));
      pw[lane] = ex / sum;
    }
    {
      if (opaque(ty) == 0) {
        const u32x4 qv = b ? qq[1] : qq[0];
        float qf[8];
#pragma unroll
        for (int d = 0; d < 4; ++d) { qf[2 * d] = bf_lo(qv[d]); qf[2 * d + 1] = bf_hi(qv[d]); }
        f32x2 x[8];
#pragma unroll
        for (int j = 0; j < 8; ++j)
#pragma unroll
          for (int i = 0; i < 8; ++i) {
            const f32x2 aa = f32x2{acc[i >> 1][j][2 * (i & 1)], acc[i >> 1][j][2 * (i & 1) + 1]}, qq2 = f32x2{qf[j], qf[j]};
            x[i] = j == 0 ? qq2 * aa : __builtin_elementwise_fma(qq2, aa, x[i]);
          }
        float z0 = x[0][0], z1 = x[0][1], z2 = x[1][0], z3 = x[1][1], y0 = x[2][0], y1 = x[2][1], y2 = x[3][0], y3 = x[3][1];
#define DA(d, s, m) "v_add_f32_dpp %" #d ", %" #s ", %" #s " row_ror:8 row_mask:0xf bank_mask:" #m "\n"
#define DB(d, s, m) "v_add_f32_dpp %" #d ", %" #s ", %" #s " row_half_mirror row_mask:0xf bank_mask:" #m "\n"
#define DQ(d, p) "v_add_f32_dpp %" #d ", %" #d ", %" #d " quad_perm:" p " row_mask:0xf bank_mask:0xf\n"
        asm("s_nop 1\n"
            DA(0, 0, 0x3) DA(1, 1, 0x3) DA(2, 2, 0x3) DA(3, 3, 0x3) DA(4, 4, 0x3) DA(5, 5, 0x3) DA(6, 6, 0x3) DA(7, 7, 0x3)
            DA(0, 8, 0xc) DA(1, 9, 0xc) DA(2, 10, 0xc) DA(3, 11, 0xc) DA(4, 12, 0xc) DA(5, 13, 0xc) DA(6, 14, 0xc) DA(7, 15, 0xc)
            DB(0, 0, 0x5) DB(1, 1, 0x5) DB(2, 2, 0x5) DB(3, 3, 0x5) DB(0, 4, 0xa) DB(1, 5, 0xa) DB(2, 6, 0xa) DB(3, 7, 0xa)
            DQ(0, "[1,0,3,2]") DQ(1, "[1,0,3,2]") DQ(2, "[1,0,3,2]") DQ(3, "[1,0,3,2]")
            DQ(0, "[2,3,0,1]") DQ(1, "[2,3,0,1]") DQ(2, "[2,3,0,1]") DQ(3, "[2,3,0,1]")
            : "+v"(z0), "+v"(z1), "+v"(z2), "+v"(z3), "+v"(y0), "+v"(y1), "+v"(y2), "+v"(y3)
            : "v"(x[4][0]), "v"(x[4][1]), "v"(x[5][0]), "v"(x[5][1]), "v"(x[6][0]), "v"(x[6][1]), "v"(x[7][0]), "v"(x[7][1]));
        if ((c & 3) == 0) *reinterpret_cast<f32x4*>(sc + wave * RK + 4 * c + 4 * g) = f32x4{z0, z1, z2, z3};
      }
      if (opaque(seg) == 2) {
        __builtin_amdgcn_fence(__ATOMIC_RELEASE, "wavefront", "local");
        __builtin_amdgcn_wave_barrier();
        __builtin_amdgcn_fence(__ATOMIC_ACQUIRE, "wavefront", "local");
        f32x2 o2[8];
#pragma unroll
        for (int t = 0; t < 4; ++t) {
          const f32x4 pv = *reinterpret_cast<const f32x4*>(pw + 16 * t + 4 * g);
#pragma unroll
          for (int h = 0; h < 2; ++h)
#pragma unroll
            for (int j = 0; j < 8; ++j) {
              const f32x2 pp = f32x2{pv[2 * h], pv[2 * h + 1]}, aa = f32x2{acc[t][j][2 * h], acc[t][j][2 * h + 1]};
              o2[j] = (t == 0 && h == 0) ? pp * aa : __builtin_elementwise_fma(pp, aa, o2[j]);
            }
        }
        float o[8];
#pragma unroll
        for (int j = 0; j < 8; ++j) { o[j] = o2[j][0] + o2[j][1]; asm("" : "+v"(o[j])); }
        const float y0 = swap16_sum(o[0], o[4]), y1 = swap16_sum(o[1], o[5]);
        const float y2 = swap16_sum(o[2], o[6]), y3 = swap16_sum(o[3], o[7]);
        *reinterpret_cast<f32x2*>(op + wave * DH + 8 * c + 4 * (g & 1) + (g & 2)) = f32x2{swap32_sum(y0, y2), swap32_sum(y1, y3)};
      }
      if (opaque(seg) == 0 || seg == 2 || seg == 3) {
#pragma unroll
        for (int t = 0; t < 4; ++t)
#pragma unroll
          for (int j = 0; j < 8; ++j) acc[t][j] = f32x4{0.f, 0.f, 0.f, 0.f};
      }
    }
  }
  {
    constexpr int SL = 2 * BPW * NS - 2;
    int tid = threadIdx.x;
    asm volatile("" : "+v"(tid));
    const int lane = tid & 63, g = lane >> 4, c = lane & 15;
    const float* pw = lds + 2 * NW * RK + wave * RK;
    float* op = lds + 3 * NW * RK + (BPW - 1) * (NW * DH);
    f32x4 pv[4];
    float pc[4];
    u32x4 P[2][4], Y[8];
#pragma unroll
    for (int t = 0; t < 4; ++t) pv[t] = *reinterpret_cast<const f32x4*>(pw + 16 * t + 4 * g);
#pragma unroll
    for (int t = 0; t < 4; ++t) pc[t] = pw[16 * t + c];
#pragma unroll
    for (int t = 0; t < 4; ++t) P[0][t] = *reinterpret_cast<const u32x4*>(smem + (rd ^ ((SL & 2) << 12)) + t * 2048);
    __builtin_amdgcn_sched_barrier(0);
    f32x2 o2[8];
#pragma unroll
    for (int t = 0; t < 4; ++t)
#pragma unroll
      for (int h = 0; h < 2; ++h)
#pragma unroll
        for (int j = 0; j < 8; ++j) {
          const f32x2 pp = f32x2{pv[t][2 * h], pv[t][2 * h + 1]}, aa = f32x2{acc[t][j][2 * h], acc[t][j][2 * h + 1]};
          o2[j] = (t == 0 && h == 0) ? pp * aa : __builtin_elementwise_fma(pp, aa, o2[j]);
        }
    asm volatile("" : "+v"(o2[0]), "+v"(o2[1]), "+v"(o2[2]), "+v"(o2[3]), "+v"(o2[4]), "+v"(o2[5]), "+v"(o2[6]), "+v"(o2[7]), "+v"(xoff));
    __builtin_amdgcn_sched_barrier(0);
#pragma unroll
    for (int t = 0; t < 4; ++t) P[1][t] = *reinterpret_cast<const u32x4*>(smem + (rd ^ (64 | ((SL & 2) << 12))) + t * 2048);
#pragma unroll
    for (int i = 0; i < 8; ++i) Y[i] = LDX(rv, XS(SL + 1), i);
    __builtin_amdgcn_sched_barrier(0);
    f32x2 o[4];
#pragma unroll
    for (int d = 0; d < 4; ++d) { float lo = o2[2 * d][0] + o2[2 * d][1], hi = o2[2 * d + 1][0] + o2[2 * d + 1][1]; asm("" : "+v"(lo), "+v"(hi)); o[d] = f32x2{lo, hi}; }
    float w[16];
#pragma unroll
    for (int s = 0; s < 2; ++s) {
      f32x2 x[4];
#pragma unroll
      for (int t = 0; t < 4; ++t)
#pragma unroll
        for (int d = 0; d < 4; ++d) {
          const f32x2 fv = f32x2{bf_lo(P[s][t][d]), bf_hi(P[s][t][d])};
          x[d] = t == 0 ? fv * pc[0] : __builtin_elementwise_fma(fv, f32x2{pc[t], pc[t]}, x[d]);
        }
#pragma unroll
      for (int i = 0; i < 8; ++i) w[8 * s + i] = x[i >> 1][i & 1];
    }
    __builtin_amdgcn_sched_barrier(0);
#pragma unroll
    for (int i = 0; i < 16; ++i) w[i] += dpp<0x128>(w[i]);
    __builtin_amdgcn_sched_barrier(0);
#pragma unroll
    for (int i = 0; i < 16; ++i) w[i] += dpp<0x124>(w[i]);
    __builtin_amdgcn_sched_barrier(0);
#pragma unroll
    for (int i = 0; i < 16; ++i) w[i] += dpp<0x122>(w[i]);
    __builtin_amdgcn_sched_barrier(0);
#pragma unroll
    for (int i = 0; i < 16; ++i) w[i] += dpp<0x121>(w[i]);
    asm volatile("" : "+v"(w[0]), "+v"(w[1]), "+v"(w[2]), "+v"(w[3]), "+v"(w[4]), "+v"(w[5]), "+v"(w[6]), "+v"(w[7]));
    asm volatile("" : "+v"(w[8]), "+v"(w[9]), "+v"(w[10]), "+v"(w[11]), "+v"(w[12]), "+v"(w[13]), "+v"(w[14]), "+v"(w[15]));
    __builtin_amdgcn_sched_barrier(0);
#pragma unroll
    for (int i = 0; i < 8; ++i) {
#pragma unroll
      for (int d = 0; d < 4; ++d)
        o[d] = __builtin_elementwise_fma(f32x2{bf_lo(X[i][d]), bf_hi(X[i][d])}, f32x2{w[i], w[i]}, o[d]);
      __builtin_amdgcn_sched_barrier(0);
    }
    asm volatile("" :: "v"(R[0]), "v"(R[1]), "v"(R[2]), "v"(R[3]));
#pragma unroll
    for (int i = 0; i < 8; ++i) {
#pragma unroll
      for (int d = 0; d < 4; ++d)
        o[d] = __builtin_elementwise_fma(f32x2{bf_lo(Y[i][d]), bf_hi(Y[i][d])}, f32x2{w[8 + i], w[8 + i]}, o[d]);
      __builtin_amdgcn_sched_barrier(0);
    }
    const float y0 = swap16_sum(o[0][0], o[2][0]), y1 = swap16_sum(o[0][1], o[2][1]);
    const float y2 = swap16_sum(o[1][0], o[3][0]), y3 = swap16_sum(o[1][1], o[3][1]);
    *reinterpret_cast<f32x2*>(op + wave * DH + 8 * c + 4 * (g & 1) + (g & 2)) = f32x2{swap32_sum(y0, y2), swap32_sum(y1, y3)};
  }
  __builtin_amdgcn_fence(__ATOMIC_RELEASE, "workgroup", "local");
  __builtin_amdgcn_s_barrier();
  __builtin_amdgcn_fence(__ATOMIC_ACQUIRE, "workgroup", "local");
  {
    const int tid = threadIdx.x;
    const float* op = lds + 3 * NW * RK + (tid >> 7) * (NW * DH) + (tid & 127);
    float r = 0.f;
#pragma unroll
    for (int w = 0; w < NW; ++w) r += op[w * DH];
    out[(size_t)bh0 * DH + tid] = __float2bfloat16(r);
  }
}
}

extern "C" void kernel_launch(void* const* d_in, const int* in_sizes, int n_in, void* d_out, int out_size, void* d_ws, size_t ws_size,
                              hipStream_t stream) {
  (void)in_sizes; (void)n_in; (void)out_size; (void)d_ws; (void)ws_size;
  lf::linf_kernel<<<lf::NBH / 2, 256, 0, stream>>>(
      (const unsigned short*)d_in[0], (const unsigned short*)d_in[1], (const unsigned short*)d_in[2],
      (const unsigned short*)d_in[3], (const unsigned short*)d_in[4], (__hip_bfloat16*)d_out);
}
```
